```python
import torch

def _softmax(x, axis=-1):
    return torch.softmax(x, dim=axis)
def _gelu(x):
    return torch.nn.functional.gelu(x, approximate="tanh")
def _silu(x):
    return torch.nn.functional.silu(x)
def _sigmoid(x):
    return torch.sigmoid(x)
def _layernorm(x, axis=-1, eps=1e-5):
    m = x.mean(dim=axis, keepdim=True)
    v = x.var(dim=axis, keepdim=True, unbiased=False)
    return (x - m) / torch.sqrt(v + eps)
def _scatter_add(tgt, idx, src, axis=0):
    return tgt.clone().index_add_(axis, idx, src)
def _segment_sum(src, seg, n):
    out = torch.zeros((n,) + src.shape[1:], dtype=src.dtype)
    return out.index_add_(0, seg, src)
def _pad(x, pw):
    flat = tuple(v for pair in reversed(pw) for v in pair)
    return torch.nn.functional.pad(x, flat)

kernel_name = 'sequential_preLN__ssm_conv_s4__dense_activation_squared_relu__prompt'

def setup_inputs():

    _w = (lambda a: (lambda t: t.to(torch.bfloat16) if t.is_floating_point() else t)(torch.as_tensor(a)))
    def _A_setup():
        B, L, D, N = 1, 2048, 512, 128
        A_re = -(lambda _x: torch.exp(torch.as_tensor(_x)))((lambda *s: torch.randn(*s, dtype=torch.bfloat16) * 3.2)(D, N))
        A_im = torch.pi * (lambda *s: torch.randn(*s, dtype=torch.bfloat16) * 0.05)(D, N)
        B_re = (lambda *s: torch.randn(*s, dtype=torch.bfloat16) * 0.1)(D, N)
        B_im = (lambda *s: torch.randn(*s, dtype=torch.bfloat16) * 0.1)(D, N)
        C_re = (lambda *s: torch.randn(*s, dtype=torch.bfloat16) * 0.1)(D, N)
        C_im = (lambda *s: torch.randn(*s, dtype=torch.bfloat16) * 0.1)(D, N)
        log_dt = (lambda *s: torch.randn(*s, dtype=torch.bfloat16) * 0.05)(D) * 0.5 - 4.0
        Dskip = (lambda *s: torch.randn(*s, dtype=torch.bfloat16) * 0.05)(D)
        return {
            "u": _w((lambda *s: torch.randn(*s, dtype=torch.bfloat16) * 0.05)(B, L, D)),
            "A_re": _w(A_re),
            "A_im": _w(A_im),
            "B_re": _w(B_re),
            "B_im": _w(B_im),
            "C_re": _w(C_re),
            "C_im": _w(C_im),
            "log_dt": _w(log_dt),
            "Dskip": _w(Dskip),
        }
    _A_in = _A_setup()

    def _B_setup():
        B, S, D, F = 1, 2048, 512, 1792
        return {
            "x": _w((lambda *s: torch.randn(*s, dtype=torch.bfloat16) * 0.05)(B, S, D)),
            "w1": _w((lambda *s: torch.randn(*s, dtype=torch.bfloat16) * 0.05)(D, F)),
            "b1": _w((lambda *s: torch.randn(*s, dtype=torch.bfloat16) * 0.2)(F)),
            "w2": _w((lambda *s: torch.randn(*s, dtype=torch.bfloat16) * 0.05)(F, D)),
            "b2": _w((lambda *s: torch.randn(*s, dtype=torch.bfloat16) * 1.6)(D)),
        }
    _B_in = _B_setup()

    return {
        "A_u": _A_in["u"],
        "A_A_re": _A_in["A_re"],
        "A_A_im": _A_in["A_im"],
        "A_B_re": _A_in["B_re"],
        "A_B_im": _A_in["B_im"],
        "A_C_re": _A_in["C_re"],
        "A_C_im": _A_in["C_im"],
        "A_log_dt": _A_in["log_dt"],
        "A_Dskip": _A_in["Dskip"],
        "B_x": _B_in["x"],
        "B_w1": _B_in["w1"],
        "B_b1": _B_in["b1"],
        "B_w2": _B_in["w2"],
        "B_b2": _B_in["b2"],
    }

def reference(A_u, A_A_re, A_A_im, A_B_re, A_B_im, A_C_re, A_C_im, A_log_dt, A_Dskip, B_x, B_w1, B_b1, B_w2, B_b2):
    def _A_ref(u, A_re, A_im, B_re, B_im, C_re, C_im, log_dt, Dskip):
        Bsz, L, D = u.shape
        dt = torch.exp(log_dt)[:, None]
        A = A_re + 1j * A_im
        Bc = B_re + 1j * B_im
        Cc = C_re + 1j * C_im
        logdA = dt * A
        dB = dt * Bc
        l = (lambda x, dt: x.to(dt))(torch.arange(L), A_re.dtype)
        P = torch.exp(logdA[:, :, None] * l[None, None, :])
        K = torch.einsum("dn,dnl,dn->dl", Cc, P, dB).real
        L2 = 2 * L
        Kp = _pad(K, ((0, 0), (0, L)))
        up = (lambda x, *d: x.permute(*d))(u, 0, 2, 1)
        up = _pad(up, ((0, 0), (0, 0), (0, L)))
        Kf = (lambda x, axes=(-2,-1): torch.fft.rfft2(x.float(), dim=axes))(Kp[:, None, :], axes=(-2, -1))
        Uf = (lambda x, axes=(-2,-1): torch.fft.rfft2(x.float(), dim=axes))(up[:, :, None, :], axes=(-2, -1))
        Yf = Uf * Kf[None, :, :, :]
        y = (lambda x, s, axes=(-2,-1): torch.fft.irfft2(x, s=s, dim=axes).to(torch.bfloat16))(Yf, (1, L2), axes=(-2, -1))[:, :, 0, :L]
        y = (lambda x, *d: x.permute(*d))(y, 0, 2, 1) + u * Dskip[None, None, :]
        return y

    def _B_ref(x, w1, b1, w2, b2):
        h = torch.einsum("bsd,df->bsf", x, w1) + b1
        zero = torch.zeros_like(h)
        r = torch.where(h > zero, h, zero)
        h = r * r
        out = torch.einsum("bsf,fd->bsd", h, w2) + b2
        return out

    x = A_u
    h = x + _A_ref(_layernorm(x, axis=-1), A_A_re, A_A_im, A_B_re, A_B_im, A_C_re, A_C_im, A_log_dt, A_Dskip)
    h2 = h + _B_ref(_layernorm(h, axis=-1), B_w1, B_b1, B_w2, B_b2)
    return h2
```

```cpp
#include <hip/hip_runtime.h>
#include <hip/hip_bf16.h>
#include <stdint.h>

#define ROUND_L 0

typedef unsigned short u16;
static constexpr int L = 2048, D = 512, N = 128, F = 1792;

__device__ __forceinline__ float bf2f(u16 v) { return __uint_as_float(((unsigned)v) << 16); }
__device__ __forceinline__ u16 f2bf(float f) {
    unsigned u = __float_as_uint(f);
    u += 0x7FFF + ((u >> 16) & 1);
    return (u16)(u >> 16);
}
__device__ __forceinline__ float rbf(float f) { return bf2f(f2bf(f)); }

__device__ float block_sum(float v, float* red) {
    for (int o = 32; o >= 1; o >>= 1) v += __shfl_xor(v, o);
    int w = threadIdx.x >> 6;
    __syncthreads();
    if ((threadIdx.x & 63) == 0) red[w] = v;
    __syncthreads();
    float s = 0;
    for (int i = 0; i < (int)(blockDim.x >> 6); i++) s += red[i];
    return s;
}

__global__ void ln_kernel(const u16* x, u16* out) {
    __shared__ float red[8];
    int t = blockIdx.x, d = threadIdx.x;
    float v = bf2f(x[t * D + d]);
    float m = block_sum(v, red) / D;
    float dv = v - m;
    float var = block_sum(dv * dv, red) / D;
    float mb = rbf(m), vb = rbf(var);
    float den = rbf(sqrtf(rbf(vb + 1e-5f)));
    out[t * D + d] = f2bf(rbf(v - mb) / den);
}

__global__ void kgen_kernel(const u16* Are, const u16* Aim, const u16* Bre, const u16* Bim, const u16* Cre, const u16* Cim,
                            const u16* logdt, float* K) {
    int d = blockIdx.y;
    int l = blockIdx.x * blockDim.x + threadIdx.x;
    float dt = rbf(expf(bf2f(logdt[d])));
    float lf = ROUND_L ? rbf((float)l) : (float)l;
    float acc = 0;
    for (int n = 0; n < N; n++) {
        float ar = dt * bf2f(Are[d * N + n]), ai = dt * bf2f(Aim[d * N + n]);
        float br = dt * bf2f(Bre[d * N + n]), bi = dt * bf2f(Bim[d * N + n]);
        float cr = bf2f(Cre[d * N + n]), ci = bf2f(Cim[d * N + n]);
        float wr = cr * br - ci * bi, wi = cr * bi + ci * br;
        float e = expf(ar * lf);
        float s, c;
        sincosf(ai * lf, &s, &c);
        acc += e * (wr * c - wi * s);
    }
    K[d * L + l] = acc;
}

__global__ void conv_kernel(const u16* x, const u16* u, const float* K, const u16* Dskip, u16* h) {
    int d = blockIdx.y;
    int t = blockIdx.x * blockDim.x + threadIdx.x;
    float acc = 0;
    for (int s = 0; s <= t; s++) acc += K[d * L + s] * bf2f(u[(t - s) * D + d]);
    float y = rbf(rbf(acc) + rbf(bf2f(u[t * D + d]) * bf2f(Dskip[d])));
    h[t * D + d] = f2bf(bf2f(x[t * D + d]) + y);
}

__global__ void gemm1_kernel(const u16* ln2, const u16* w1, const u16* b1, u16* a) {
    int t = blockIdx.y;
    int f = blockIdx.x * blockDim.x + threadIdx.x;
    float acc = 0;
    for (int k = 0; k < D; k++) acc += bf2f(ln2[t * D + k]) * bf2f(w1[k * F + f]);
    float hh = rbf(rbf(acc) + bf2f(b1[f]));
    float r = hh > 0 ? hh : 0;
    a[t * F + f] = f2bf(r * r);
}

__global__ void gemm2_kernel(const u16* a, const u16* w2, const u16* b2, const u16* h, u16* out) {
    int t = blockIdx.y;
    int d = blockIdx.x * blockDim.x + threadIdx.x;
    float acc = 0;
    for (int k = 0; k < F; k++) acc += bf2f(a[t * F + k]) * bf2f(w2[k * D + d]);
    float o = rbf(rbf(acc) + bf2f(b2[d]));
    out[t * D + d] = f2bf(bf2f(h[t * D + d]) + o);
}

extern "C" void kernel_launch(void* const* d_in, const int* in_sizes, int n_in,
                              void* d_out, int out_size, void* d_ws, size_t ws_size,
                              hipStream_t stream) {
    const u16* x = (const u16*)d_in[0];
    const u16* Are = (const u16*)d_in[1];
    const u16* Aim = (const u16*)d_in[2];
    const u16* Bre = (const u16*)d_in[3];
    const u16* Bim = (const u16*)d_in[4];
    const u16* Cre = (const u16*)d_in[5];
    const u16* Cim = (const u16*)d_in[6];
    const u16* logdt = (const u16*)d_in[7];
    const u16* Dskip = (const u16*)d_in[8];
    const u16* w1 = (const u16*)d_in[10];
    const u16* b1 = (const u16*)d_in[11];
    const u16* w2 = (const u16*)d_in[12];
    const u16* b2 = (const u16*)d_in[13];
    char* ws = (char*)d_ws;
    u16* u = (u16*)ws;
    float* K = (float*)(ws + (4 << 20));
    u16* h = (u16*)(ws + (8 << 20));
    u16* ln2 = (u16*)(ws + (12 << 20));
    u16* a = (u16*)(ws + (16 << 20));
    ln_kernel<<<L, D, 0, stream>>>(x, u);
    kgen_kernel<<<dim3(L / 256, D), 256, 0, stream>>>(Are, Aim, Bre, Bim, Cre, Cim, logdt, K);
    conv_kernel<<<dim3(L / 256, D), 256, 0, stream>>>(x, u, K, Dskip, h);
    ln_kernel<<<L, D, 0, stream>>>(h, ln2);
    gemm1_kernel<<<dim3(F / 256, L), 256, 0, stream>>>(ln2, w1, b1, a);
    gemm2_kernel<<<dim3(D / 256, L), 256, 0, stream>>>(a, w2, b2, h, (u16*)d_out);
}
```

```cpp
#include <hip/hip_runtime.h>
#include <stdint.h>

typedef unsigned u32;
typedef unsigned short u16;
typedef u32 v4u __attribute__((ext_vector_type(4)));
typedef u32 v2u __attribute__((ext_vector_type(2)));
typedef float f4 __attribute__((ext_vector_type(4)));
typedef float f2 __attribute__((ext_vector_type(2)));
typedef __bf16 bf8 __attribute__((ext_vector_type(8)));
typedef __bf16 bf2 __attribute__((ext_vector_type(2)));
typedef short s4 __attribute__((ext_vector_type(4)));
typedef short s2 __attribute__((ext_vector_type(2)));
#define LAS __attribute__((address_space(3)))
#define GAS __attribute__((address_space(1)))
#define DEV __device__ __forceinline__

static constexpr int L = 2048, D = 512, NS = 128, F = 1792;
static constexpr int SC1 = 16;
static constexpr u32 SPIN_CAP = 1u << 18;

static constexpr u32 WS_EPOCH = 0, WS_FLAG = 4096, WS_STATS = 16384, WS_HP = 1u << 20, WS_XG = 4u << 20, WS_A2 = 8u << 20,
                     WS_PROF = 16u << 20, WS_TOTAL = 17u << 20;
static constexpr u32 T_S1 = 53, T_S2 = 432, T_CH = 4 * T_S2 * 16;
static constexpr u32 LD_BIG = 0, LD_KIMG = 114688, KIMG_SZ = 4160, LD_UIMG = LD_KIMG + 4 * KIMG_SZ, UIMG_SZ = 4608,
                     LD_KT = LD_UIMG + 2 * UIMG_SZ, LD_EX = LD_KIMG, LD_CS = 155648, LD_RI = LD_CS + 4096, LDS_TOTAL = 163840;

DEV f4 mfma(v4u a, v4u b, f4 c) { return __builtin_amdgcn_mfma_f32_16x16x32_bf16(__builtin_bit_cast(bf8, a), __builtin_bit_cast(bf8, b), c, 0, 0, 0); }
DEV u32 pk2(float lo, float hi) { f2 z = {lo, hi}; return __builtin_bit_cast(u32, __builtin_convertvector(z, bf2)); }
DEV float blo(u32 v) { return __uint_as_float(v << 16); }
DEV float bhi(u32 v) { return __uint_as_float(v & 0xffff0000u); }
DEV float rb(float v) { return blo(pk2(v, v)); }
template <int CTRL> DEV float dppadd(float v) { return v + __uint_as_float(__builtin_amdgcn_update_dpp(0u, __float_as_uint(v), CTRL, 0xf, 0xf, true)); }
DEV float swap16add(float v) { auto t = __builtin_amdgcn_permlane16_swap(__float_as_uint(v), __float_as_uint(v), false, false); return __uint_as_float(t[0]) + __uint_as_float(t[1]); }
DEV float swap32add(float v) { auto t = __builtin_amdgcn_permlane32_swap(__float_as_uint(v), __float_as_uint(v), false, false); return __uint_as_float(t[0]) + __uint_as_float(t[1]); }
DEV float red32(float v) { v = dppadd<0xB1>(v); v = dppadd<0x4E>(v); v = dppadd<0x141>(v); v = dppadd<0x140>(v); return swap16add(v); }
DEV f2 cmul(f2 a, f2 b) { f2 s = {-a.y, a.x}; f2 br = {b.x, b.x}, bi = {b.y, b.y}; return a * br + s * bi; }
DEV f2 cexpm(float lr, float ph, float m) {
    float mag = __builtin_amdgcn_exp2f(lr * m), th = __builtin_amdgcn_fractf(ph * m);
    f2 z = {mag * __builtin_amdgcn_cosf(th), mag * __builtin_amdgcn_sinf(th)};
    return z;
}
DEV void lds_barrier() { asm volatile("s_waitcnt lgkmcnt(0)\n\ts_barrier" ::: "memory"); }
DEV void glds(const void* base, u32 voff, u32 dst) {
    asm volatile("s_mov_b32 m0, %2\n\ts_nop 0\n\tglobal_load_lds_dwordx4 %0, %1" ::"v"(voff), "s"(base), "s"(dst) : "memory");
}
DEV v2u trread(u32 addr) { return __builtin_bit_cast(v2u, __builtin_amdgcn_ds_read_tr16_b64_v4i16((LAS s4*)addr)); }
template <typename T> DEV T ldsr(u32 addr) { return *(LAS T*)addr; }
template <typename T> DEV void ldsw(u32 addr, T v) { *(LAS T*)addr = v; }

#define STAMP(i)

__global__ void __launch_bounds__(256, 1)
fused_kernel(const u16* __restrict__ x, const u16* __restrict__ Are, const u16* __restrict__ Aim, const u16* __restrict__ Bre,
             const u16* __restrict__ Bim, const u16* __restrict__ Cre, const u16* __restrict__ Cim, const u16* __restrict__ logdt,
             const u16* __restrict__ Dskip, const u16* __restrict__ w1, const u16* __restrict__ b1, const u16* __restrict__ w2,
             const u16* __restrict__ b2, u16* __restrict__ out, char* ws) {
    __shared__ __attribute__((aligned(16))) char smem[LDS_TOTAL];
    const u32 lb = (u32)(uintptr_t)(LAS char*)smem;
    const int b = blockIdx.x, tid = threadIdx.x, lane = tid & 63, w = __builtin_amdgcn_readfirstlane(tid >> 6);
    const int r = lane & 15, g = lane >> 4;
    const int mt = 2 * (b & 7) + ((b >> 3) & 1), nt = b >> 4;
    const __amdgpu_buffer_rsrc_t rs = __builtin_amdgcn_make_buffer_rsrc(ws, 0, WS_TOTAL, 0x00020000);
    STAMP(0);
    {
        const v4u ones = {~0u, ~0u, ~0u, ~0u};
        __builtin_amdgcn_raw_buffer_store_b128(ones, rs, WS_HP + b * 8192 + w * 2048 + lane * 16, 0, SC1);
        __builtin_amdgcn_raw_buffer_store_b128(ones, rs, WS_HP + b * 8192 + w * 2048 + 1024 + lane * 16, 0, SC1);
    }
    asm volatile("" ::: "memory");
    const u32 ep = __builtin_amdgcn_raw_buffer_load_b32(rs, WS_EPOCH, 0, SC1);
    const int srow = 8 * b + 2 * w + (lane >> 5);
    const v4u xr0 = ((const v4u*)x)[srow * 64 + (lane & 31)], xr1 = ((const v4u*)x)[srow * 64 + 32 + (lane & 31)];
    const int trow = 64 * (b >> 3) + 16 * w + 2 * (lane & 7), tpair = 32 * (b & 7) + 4 * (lane >> 3);
    const v4u xt0 = ((const v4u*)x)[trow * 64 + (tpair >> 2)], xt1 = ((const v4u*)x)[trow * 64 + 64 + (tpair >> 2)];
    const int ch = w >> 1, pidx = (2 * b) * NS + tid;
    const u32 pAr = Are[pidx], pAi = Aim[pidx], pBr = Bre[pidx], pBi = Bim[pidx], pCr = Cre[pidx], pCi = Cim[pidx];
    const u32 pdt = logdt[2 * b + ch];
    const u32 dsk = ((const u32*)Dskip)[b];
    if (tid < 60) { const v4u z = {0, 0, 0, 0}; ldsw<v4u>(lb + LD_UIMG + (tid >= 30 ? UIMG_SZ : 0) + (tid % 30) * 16, z); }
    else if (tid >= 64 && tid < 72) { const v4u z = {0, 0, 0, 0}; ldsw<v4u>(lb + LD_KIMG + ((tid - 64) >> 1) * KIMG_SZ + 4096 + (tid & 1) * 16, z); }
    const u32 tag = __builtin_amdgcn_readfirstlane(ep) + 1u;
    {
        float v[16];
#pragma unroll
        for (int i = 0; i < 4; i++) { v[2 * i] = blo(xr0[i]); v[2 * i + 1] = bhi(xr0[i]); v[8 + 2 * i] = blo(xr1[i]); v[9 + 2 * i] = bhi(xr1[i]); }
        float s = 0;
#pragma unroll
        for (int i = 0; i < 16; i++) s += v[i];
        const float mean = red32(s) * (1.0f / D);
        float q = 0;
#pragma unroll
        for (int i = 0; i < 16; i++) { float dv = v[i] - mean; q += dv * dv; }
        const float var = red32(q) * (1.0f / D);
        const float den = __builtin_amdgcn_sqrtf(rb(rb(var) + 1e-5f));
        const v2u gr = {pk2(mean, den), tag};
        if ((lane & 31) == 0) __builtin_amdgcn_raw_buffer_store_b64(gr, rs, WS_STATS + srow * 8, 0, SC1);
#pragma unroll
        for (int i = 0; i < 4; i++) {
            const v4u o = {xt0[i], tag, xt1[i], tag};
            __builtin_amdgcn_raw_buffer_store_b128(o, rs, WS_XG + ((tpair + i) * L + trow) * 8, 0, SC1);
        }
    }
    STAMP(1);
    {
        const int n = tid & 127;
        const float dt = rb(expf(blo(pdt)));
        const float lr = dt * blo(pAr) * 1.44269504088896341f, ph = dt * blo(pAi) * 0.15915494309189535f;
        f2 cc = {blo(pCr), blo(pCi)}, db = {dt * blo(pBr), dt * blo(pBi)};
        const u32 tb = lb + LD_BIG + ch * T_CH + ((n >> 5) * T_S2 + ((n >> 2) & 7) * T_S1) * 16 + (n & 3) * 4;
        const f2 E1 = cexpm(lr, ph, 1.f), E8 = cexpm(lr, ph, 8.f), E16 = cexpm(lr, ph, 16.f), E256 = cexpm(lr, ph, 256.f);
        f2 ya = {1.f, 0.f}, yb = E8;
#pragma unroll
        for (int j = 0; j < 16; j++) {
            ldsw<u32>(tb + (2 * j) * 16, pk2(ya.x, ya.y));
            ldsw<u32>(tb + (2 * j + 1) * 16, pk2(yb.x, yb.y));
            if (j < 15) { ya = (j == 0) ? E16 : cmul(ya, E16); yb = cmul(yb, E16); }
        }
        f2 X[21];
        X[0] = cmul(cc, db);
#pragma unroll
        for (int e = 1; e < 8; e++) X[e] = cmul(X[e - 1], E1);
#pragma unroll
        for (int e = 0; e < 4; e++) X[8 + e] = cmul(X[2 * e], E256);
        X[12] = cmul(X[8], E256); X[13] = cmul(X[10], E256); X[14] = cmul(X[12], E256); X[15] = cmul(X[13], E256);
        X[16] = cmul(X[14], E256);
#pragma unroll
        for (int e = 17; e < 21; e++) X[e] = cmul(X[e - 1], E256);
#pragma unroll
        for (int e = 0; e < 21; e++) ldsw<u32>(tb + (32 + e) * 16, pk2(X[e].x, -X[e].y));
        lds_barrier();
        const int jt = w & 1;
        const u32 fb = lb + LD_BIG + ch * T_CH + (g * T_S2 + r) * 16;
        f4 k0 = {0, 0, 0, 0}, k1 = {0, 0, 0, 0};
#pragma unroll
        for (int ks = 0; ks < 8; ks++) {
            const v4u yf = ldsr<v4u>(fb + (ks * T_S1 + 16 * jt) * 16), xa = ldsr<v4u>(fb + (ks * T_S1 + 32) * 16), xb = ldsr<v4u>(fb + (ks * T_S1 + 48) * 16);
            k0 = mfma(xa, yf, k0); k1 = mfma(xb, yf, k1);
        }
        const int j = 16 * jt + r;
        const u32 kt = lb + LD_KT + ch * 2048;
        const u32 a0 = g < 2 ? (8 * j + 4 * g) : (g == 2 ? 256 + 4 * j : 384 + 2 * j), a1 = g < 3 ? a0 + 2 : a0 + 64;
        ldsw<u32>(kt + a0 * 2, pk2(k0[0], k0[1]));
        ldsw<u32>(kt + a1 * 2, pk2(k0[2], k0[3]));
#pragma unroll
        for (int i = 0; i < 4; i++) ldsw<u16>(kt + (512 + 32 * (4 * g + i) + j) * 2, (u16)pk2(k1[i], k1[i]));
        lds_barrier();
#pragma unroll
        for (int c2 = 0; c2 < 2; c2++) {
            const int m = n + 128 * c2;
            u32 v[9];
#pragma unroll
            for (int e = 0; e < 9; e++) {
                const int s = 2047 - 8 * m - e;
                const u32 idx = s < 128 ? (u32)max(s, 0) : (pk2((float)s, 0.f) - 0x4300u + 128u);
                v[e] = ldsr<u16>(kt + idx * 2);
            }
            if (m == 255) v[8] = 0;
            const v4u c1 = {v[0] | (v[1] << 16), v[2] | (v[3] << 16), v[4] | (v[5] << 16), v[6] | (v[7] << 16)};
            const v4u c0 = {v[1] | (v[2] << 16), v[3] | (v[4] << 16), v[5] | (v[6] << 16), v[7] | (v[8] << 16)};
            ldsw<v4u>(lb + LD_KIMG + (2 * ch) * KIMG_SZ + m * 16, c0);
            ldsw<v4u>(lb + LD_KIMG + (2 * ch + 1) * KIMG_SZ + m * 16, c1);
        }
    }
    STAMP(2);
    u32 xp[8], up[8];
    {
        v4u gx[4], gs[4];
        const u32 tok0 = 512 * w + 16 * r + 4 * g;
        for (u32 spins = 0;; ++spins) {
            asm volatile("" ::: "memory");
#pragma unroll
            for (int i = 0; i < 4; i++) {
                gs[i] = __builtin_amdgcn_raw_buffer_load_b128(rs, tok0 * 8 + (i >> 1) * 2048 + (i & 1) * 16, WS_STATS, SC1);
                gx[i] = __builtin_amdgcn_raw_buffer_load_b128(rs, tok0 * 8 + (i >> 1) * 2048 + (i & 1) * 16, WS_XG + b * (L * 8), SC1);
            }
            u32 bad = 0;
#pragma unroll
            for (int i = 0; i < 4; i++) bad |= (gs[i][1] ^ tag) | (gs[i][3] ^ tag) | (gx[i][1] ^ tag) | (gx[i][3] ^ tag);
            if (__all(bad == 0) || spins > SPIN_CAP) break;
        }
        STAMP(3);
#pragma unroll
        for (int i = 0; i < 8; i++) {
            const u32 xv = gx[i >> 1][2 * (i & 1)], sv = gs[i >> 1][2 * (i & 1)];
            const float m = blo(sv), ri = __builtin_amdgcn_rcpf(bhi(sv));
            const u32 t = pk2(blo(xv) - m, bhi(xv) - m);
            xp[i] = xv;
            up[i] = pk2(blo(t) * ri, bhi(t) * ri);
        }
#pragma unroll
        for (int e = 0; e < 2; e++) {
            const u32 ua = lb + LD_UIMG + (240 + tok0 + 256 * e) * 2;
            const v2u c0 = {__builtin_amdgcn_perm(up[4 * e + 1], up[4 * e], 0x05040100u), __builtin_amdgcn_perm(up[4 * e + 3], up[4 * e + 2], 0x05040100u)};
            const v2u c1 = {__builtin_amdgcn_perm(up[4 * e + 1], up[4 * e], 0x07060302u), __builtin_amdgcn_perm(up[4 * e + 3], up[4 * e + 2], 0x07060302u)};
            ldsw<v2u>(ua, c0);
            ldsw<v2u>(ua + UIMG_SZ, c1);
        }
    }
    lds_barrier();
    if (b == 0 && tid == 0) __builtin_amdgcn_raw_buffer_store_b32(tag, rs, WS_EPOCH, 0, SC1);
    {
        u32 vo[7];
#pragma unroll
        for (int i = 0; i < 7; i++) { const u32 o = 1024 * i + 16 * lane; vo[i] = (o / 224) * (F * 2) + (o % 224); }
        const char* src = (const char*)w1 + (size_t)(128 * w) * (F * 2) + nt * 224;
#pragma unroll
        for (int q = 0; q < 4; q++)
#pragma unroll
            for (int i = 0; i < 7; i++) glds(src + q * 32 * (F * 2), vo[i], lb + LD_BIG + w * 28672 + q * 7168 + i * 1024);
    }
    STAMP(4);
    {
        f4 acc[8];
#pragma unroll
        for (int c = 0; c < 8; c++) acc[c] = (f4){0, 0, 0, 0};
        const int qh = w & 1;
        const u32 ab = lb + LD_KIMG + (2 * ch + (r & 1)) * KIMG_SZ + (2046 - 16 - 2 * (r >> 1) + 8 * g - 128 * qh) * 2;
        const u32 bb = lb + LD_UIMG + ch * UIMG_SZ + (240 + 16 * r - 16 + 8 * g - 128 * qh) * 2;
#pragma unroll
        for (int q = 0; q < 4; q++) {
            v4u B[8];
#pragma unroll
            for (int c = 0; c < 8; c++) B[c] = ldsr<v4u>(bb + (256 * c - 32 * q) * 2);
#pragma unroll
            for (int p = 0; p < 8; p++) {
                const u32 a = ab - (32 * q + 256 * p) * 2;
                const v4u A = {ldsr<u32>(a), ldsr<u32>(a + 4), ldsr<u32>(a + 8), ldsr<u32>(a + 12)};
#pragma unroll
                for (int c = p; c < 8; c++) acc[c] = mfma(A, B[c - p], acc[c]);
            }
        }
        lds_barrier();
#pragma unroll
        for (int c = 0; c < 8; c++) ldsw<f4>(lb + LD_EX + (w * 8 + c) * 1024 + lane * 16, acc[c]);
        lds_barrier();
        const float d0 = blo(dsk), d1 = bhi(dsk);
#pragma unroll
        for (int e = 0; e < 2; e++) {
            const u32 a = lb + LD_EX + (2 * w + e) * 1024 + lane * 16;
            const f4 y0 = ldsr<f4>(a) + ldsr<f4>(a + 8192), y1 = ldsr<f4>(a + 16384) + ldsr<f4>(a + 24576);
            v4u o;
#pragma unroll
            for (int i = 0; i < 4; i++) {
                const u32 yv = pk2(y0[i], y1[i]), ud = pk2(blo(up[4 * e + i]) * d0, bhi(up[4 * e + i]) * d1);
                const u32 y2 = pk2(blo(yv) + blo(ud), bhi(yv) + bhi(ud));
                o[i] = min(pk2(blo(xp[4 * e + i]) + blo(y2), bhi(xp[4 * e + i]) + bhi(y2)), 0xFFFFFFFEu);
            }
            __builtin_amdgcn_raw_buffer_store_b128(o, rs, WS_HP + b * 8192 + (512 * w + 256 * e + 16 * r + 4 * g) * 4, 0, SC1);
        }
    }
    STAMP(5);
    const u32 wtr = lb + LD_BIG + (4 * g + (r >> 2)) * 224 + (r & 3) * 8;
    u32 b1v[4];
#pragma unroll
    for (int i = 0; i < 4; i++) b1v[i] = (4 * g + i < 7) ? b1[112 * nt + 16 * (4 * g + i) + r] : 0;
    asm volatile("s_waitcnt vmcnt(0)" ::: "memory");
    {
        f4 cs = {0, 0, 0, 0};
#pragma unroll
        for (int k4 = 0; k4 < 4; k4++)
#pragma unroll
            for (int n = 0; n < 7; n++) {
                const u32 a = wtr + (4 * w + k4) * 7168 + n * 32;
                const v2u lo = trread(a), hi = trread(a + 3584);
                const u32 one = (r == n) ? 0x3F803F80u : 0u;
                cs = mfma((v4u){one, one, one, one}, (v4u){lo[0], lo[1], hi[0], hi[1]}, cs);
            }
        ldsw<f4>(lb + LD_CS + w * 1024 + lane * 16, cs);
    }
    STAMP(6);
    v4u raw[32];
    {
        const u32 vo = (2 * g + (r >> 3)) * 8192 + (128 * mt + 32 * w + 4 * (r & 7)) * 4;
        for (u32 spins = 0;; ++spins) {
            asm volatile("" ::: "memory");
#pragma unroll
            for (int i = 0; i < 32; i++) raw[i] = __builtin_amdgcn_raw_buffer_load_b128(rs, vo, WS_HP + (i * 8) * 8192, SC1);
            u32 mx = 0;
#pragma unroll
            for (int i = 0; i < 32; i++) mx = max(max(mx, max(raw[i][0], raw[i][1])), max(raw[i][2], raw[i][3]));
            if (__all(mx != 0xFFFFFFFFu) || spins > SPIN_CAP) break;
        }
    }
    lds_barrier();
    STAMP(7);
    {
        f4 acc[7][2], sq[2], sm[2];
#pragma unroll
        for (int n = 0; n < 7; n++) { acc[n][0] = (f4){0, 0, 0, 0}; acc[n][1] = (f4){0, 0, 0, 0}; }
        sq[0] = sq[1] = sm[0] = sm[1] = (f4){0, 0, 0, 0};
        const v4u ones = {0x3F803F80u, 0x3F803F80u, 0x3F803F80u, 0x3F803F80u};
#pragma unroll
        for (int ks = 0; ks < 16; ks++) {
            v4u fr[2];
#pragma unroll
            for (int e = 0; e < 2; e++)
#pragma unroll
                for (int hf = 0; hf < 2; hf++) {
                    const v4u t = raw[2 * ks + hf];
                    fr[e][2 * hf] = __builtin_amdgcn_update_dpp(t[e], t[2 + e], 0x128, 0xf, 0xc, false);
                    fr[e][2 * hf + 1] = __builtin_amdgcn_update_dpp(t[2 + e], t[e], 0x128, 0xf, 0x3, false);
                }
#pragma unroll
            for (int e = 0; e < 2; e++) { sq[e] = mfma(fr[e], fr[e], sq[e]); sm[e] = mfma(ones, fr[e], sm[e]); }
#pragma unroll
            for (int n = 0; n < 7; n++) {
                const u32 a = wtr + ks * 7168 + n * 32;
                const v2u lo = trread(a), hi = trread(a + 3584);
                const v4u wf = {lo[0], lo[1], hi[0], hi[1]};
                acc[n][0] = mfma(wf, fr[0], acc[n][0]);
                acc[n][1] = mfma(wf, fr[1], acc[n][1]);
            }
        }
        f4 cs = ldsr<f4>(lb + LD_CS + lane * 16) + ldsr<f4>(lb + LD_CS + 1024 + lane * 16) + ldsr<f4>(lb + LD_CS + 2048 + lane * 16) + ldsr<f4>(lb + LD_CS + 3072 + lane * 16);
        v4u st[2];
#pragma unroll
        for (int e = 0; e < 2; e++) {
            float q = 0;
#pragma unroll
            for (int i = 0; i < 4; i++) q += (4 * g + i == r) ? sq[e][i] : 0.f;
            q = swap32add(swap16add(q));
            const float mean = sm[e][0] * (1.0f / D);
            const float var = fmaxf(q * (1.0f / D) - mean * mean, 0.f);
            const float den = rb(__builtin_amdgcn_sqrtf(rb(rb(var) + 1e-5f)));
            st[e] = (v4u){pk2(-mean, -mean), pk2(den, 0.f), 0u, 0u};
            const float ri = __builtin_amdgcn_rcpf(den);
            if (g == 0) ldsw<float>(lb + LD_RI + ((2 * w + e) * 16 + r) * 4, ri * ri);
        }
#pragma unroll
        for (int n = 0; n < 7; n++) {
            const float c = cs[n & 3], chi = rb(c);
            const bool on = (g == (n >> 2));
            const v4u af = {on ? pk2(chi, c - chi) : 0u, on ? b1v[n & 3] : 0u, 0u, 0u};
            acc[n][0] = mfma(af, st[0], acc[n][0]);
            acc[n][1] = mfma(af, st[1], acc[n][1]);
        }
#pragma unroll
        for (int e = 0; e < 2; e++) {
            u32 pk[14];
#pragma unroll
            for (int n = 0; n < 7; n++)
#pragma unroll
                for (int i = 0; i < 2; i++) {
                    const float u0 = acc[n][e][2 * i], u1 = acc[n][e][2 * i + 1];
                    const s2 t = __builtin_bit_cast(s2, pk2(u0 * fabsf(u0), u1 * fabsf(u1)));
                    pk[2 * n + i] = __builtin_bit_cast(u32, __builtin_elementwise_max(t, (s2){0, 0}));
                }
            const u32 base = WS_A2 + ((8 * mt + 2 * w + e) * 56 + 7 * (nt >> 1)) * 1024;
#pragma unroll
            for (int m = 0; m < 3; m++)
                __builtin_amdgcn_raw_buffer_store_b128((v4u){pk[4 * m], pk[4 * m + 1], pk[4 * m + 2], pk[4 * m + 3]}, rs, lane * 16, base + (3 * (nt & 1) + m) * 1024, SC1);
            __builtin_amdgcn_raw_buffer_store_b64((v2u){pk[12], pk[13]}, rs, lane * 8, base + 6 * 1024 + (nt & 1) * 512, SC1);
        }
    }
    STAMP(8);
    asm volatile("s_waitcnt vmcnt(0)" ::: "memory");
    if (lane == 0) __builtin_amdgcn_raw_buffer_store_b32(tag, rs, WS_FLAG + ((mt * 2 + (w >> 1)) * 32 + nt * 2 + (w & 1)) * 4, 0, SC1);
    lds_barrier();
    STAMP(9);
    const int half = nt & 1, dtile = nt >> 1;
    v4u wf2[14][4];
    {
        const u32 row = lane >> 3, chunk = (lane & 7) ^ (((row >> 1) & 3) << 1);
        const u32 vo = row * (D * 2) + chunk * 16;
        const char* src = (const char*)w2 + (size_t)(448 * w) * (D * 2) + dtile * 128;
        const u32 rrow = 4 * g + (r >> 2);
        const u32 tb = lb + LD_BIG + w * 28672 + rrow * 128 + (r & 1) * 8;
#pragma unroll
        for (int hf = 0; hf < 2; hf++) {
#pragma unroll
            for (int i = 0; i < 28; i++) glds(src + (224 * hf + 8 * i) * (D * 2), vo, lb + LD_BIG + w * 28672 + i * 1024);
            asm volatile("s_waitcnt vmcnt(0)" ::: "memory");
#pragma unroll
            for (int kk = 0; kk < 7; kk++) {
                const int r0 = kk < 3 ? 32 * kk : (kk < 6 ? 112 + 32 * (kk - 3) : 96), r1 = kk < 6 ? r0 + 16 : 208;
#pragma unroll
                for (int i = 0; i < 4; i++) {
                    const u32 co = (((2 * i + ((r >> 1) & 1)) ^ (((rrow >> 1) & 3) << 1)) * 16);
                    const v2u lo = trread(tb + r0 * 128 + co), hi = trread(tb + r1 * 128 + co);
                    wf2[7 * hf + kk][i] = (v4u){lo[0], lo[1], hi[0], hi[1]};
                }
            }
            asm volatile("s_waitcnt lgkmcnt(0)" ::: "memory");
        }
    }
    const int tokf = 128 * mt + 64 * half + 32 * (w >> 1) + 4 * (r & 7) + 2 * (r >> 3) + (w & 1);
    u32 hres[8];
    v2u b2v[4];
#pragma unroll
    for (int i = 0; i < 8; i++) hres[i] = __builtin_amdgcn_raw_buffer_load_b32(rs, (2 * g) * 8192 + tokf * 4, WS_HP + (32 * dtile + 8 * (i >> 1) + (i & 1)) * 8192, SC1);
#pragma unroll
    for (int i = 0; i < 4; i++) b2v[i] = *(const v2u*)(b2 + 64 * dtile + 16 * i + 4 * g);
    STAMP(10);
    {
        const u32 fo = WS_FLAG + ((mt * 2 + half) * 32 + 8 * w + (lane & 7)) * 4;
        for (u32 spins = 0;; ++spins) {
            asm volatile("" ::: "memory");
            const u32 v = __builtin_amdgcn_raw_buffer_load_b32(rs, fo, 0, SC1);
            if (__all(v == tag) || spins > SPIN_CAP) break;
        }
        asm volatile("" ::: "memory");
    }
    STAMP(11);
    {
        lds_barrier();
#pragma unroll
        for (int t = 0; t < 4; t++) {
            const u32 base = WS_A2 + ((8 * mt + 4 * half + t) * 56 + 14 * w) * 1024;
            v4u af[14];
#pragma unroll
            for (int kk = 0; kk < 14; kk++) {
                if (kk % 7 < 6) af[kk] = __builtin_amdgcn_raw_buffer_load_b128(rs, lane * 16, base + kk * 1024, SC1);
                else {
                    const v2u lo = __builtin_amdgcn_raw_buffer_load_b64(rs, lane * 8, base + kk * 1024, SC1), hi = __builtin_amdgcn_raw_buffer_load_b64(rs, lane * 8, base + kk * 1024 + 512, SC1);
                    af[kk] = (v4u){lo[0], lo[1], hi[0], hi[1]};
                }
            }
            f4 acc[4];
#pragma unroll
            for (int i = 0; i < 4; i++) acc[i] = (f4){0, 0, 0, 0};
#pragma unroll
            for (int kk = 0; kk < 14; kk++)
#pragma unroll
                for (int i = 0; i < 4; i++) acc[i] = mfma(wf2[kk][i], af[kk], acc[i]);
#pragma unroll
            for (int i = 0; i < 4; i++) ldsw<f4>(lb + LD_BIG + ((t * 4 + w) * 4 + i) * 1024 + lane * 16, acc[i]);
        }
        lds_barrier();
        STAMP(12);
        const float ri2 = ldsr<float>(lb + LD_RI + ((4 * half + w) * 16 + r) * 4);
#pragma unroll
        for (int i = 0; i < 4; i++) {
            const u32 a = lb + LD_BIG + (w * 16 + i) * 1024 + lane * 16;
            const f4 s = (ldsr<f4>(a) + ldsr<f4>(a + 4096) + ldsr<f4>(a + 8192) + ldsr<f4>(a + 12288)) * ri2;
            v2u o;
#pragma unroll
            for (int k = 0; k < 2; k++) {
                const u32 t0 = pk2(s[2 * k], s[2 * k + 1]);
                const u32 t1 = pk2(blo(t0) + blo(b2v[i][k]), bhi(t0) + bhi(b2v[i][k]));
                o[k] = pk2(blo(hres[2 * i + k]) + blo(t1), bhi(hres[2 * i + k]) + bhi(t1));
            }
            *(v2u*)(out + (size_t)tokf * D + 64 * dtile + 16 * i + 4 * g) = o;
        }
    }
    STAMP(13);
}

extern "C" void kernel_launch(void* const* d_in, const int* in_sizes, int n_in, void* d_out, int out_size, void* d_ws, size_t ws_size,
                              hipStream_t stream) {
    fused_kernel<<<256, 256, 0, stream>>>((const u16*)d_in[0], (const u16*)d_in[1], (const u16*)d_in[2], (const u16*)d_in[3], (const u16*)d_in[4],
                                          (const u16*)d_in[5], (const u16*)d_in[6], (const u16*)d_in[7], (const u16*)d_in[8], (const u16*)d_in[10],
                                          (const u16*)d_in[11], (const u16*)d_in[12], (const u16*)d_in[13], (u16*)d_out, (char*)d_ws);
}
```

```cpp
#include <hip/hip_runtime.h>
#include <stdint.h>

typedef unsigned u32;
typedef unsigned short u16;
typedef u32 v4u __attribute__((ext_vector_type(4)));
typedef u32 v2u __attribute__((ext_vector_type(2)));
typedef float f4 __attribute__((ext_vector_type(4)));
typedef float f2 __attribute__((ext_vector_type(2)));
typedef __bf16 bf8 __attribute__((ext_vector_type(8)));
typedef __bf16 bf2 __attribute__((ext_vector_type(2)));
typedef short s4 __attribute__((ext_vector_type(4)));
typedef short s2 __attribute__((ext_vector_type(2)));
#define LAS __attribute__((address_space(3)))
#define GAS __attribute__((address_space(1)))
#define DEV __device__ __forceinline__

static constexpr int L = 2048, D = 512, NS = 128, F = 1792;
static constexpr int SC1 = 16;
static constexpr u32 SPIN_CAP = 1u << 18;

static constexpr u32 WS_EPOCH = 0, WS_FLAG = 4096, WS_STATS = 16384, WS_HP = 1u << 20, WS_XG = 4u << 20, WS_A2 = 8u << 20,
                     WS_PROF = 16u << 20, WS_TOTAL = 17u << 20;
static constexpr u32 T_S1 = 53, T_S2 = 432, T_CH = 4 * T_S2 * 16;
static constexpr u32 LD_BIG = 0, LD_KIMG = 114688, KIMG_SZ = 4160, LD_UIMG = LD_KIMG + 4 * KIMG_SZ, UIMG_SZ = 4608,
                     LD_KT = LD_UIMG + 2 * UIMG_SZ, LD_EX = LD_KIMG, LD_CS = 155648, LD_RI = LD_CS + 4096, LDS_TOTAL = 163840;

DEV f4 mfma(v4u a, v4u b, f4 c) { return __builtin_amdgcn_mfma_f32_16x16x32_bf16(__builtin_bit_cast(bf8, a), __builtin_bit_cast(bf8, b), c, 0, 0, 0); }
DEV u32 pk2(float lo, float hi) { f2 z = {lo, hi}; return __builtin_bit_cast(u32, __builtin_convertvector(z, bf2)); }
DEV float blo(u32 v) { return __uint_as_float(v << 16); }
DEV float bhi(u32 v) { return __uint_as_float(v & 0xffff0000u); }
DEV float rb(float v) { return blo(pk2(v, v)); }
template <int CTRL> DEV float dppadd(float v) { return v + __uint_as_float(__builtin_amdgcn_update_dpp(0u, __float_as_uint(v), CTRL, 0xf, 0xf, true)); }
DEV float swap16add(float v) { auto t = __builtin_amdgcn_permlane16_swap(__float_as_uint(v), __float_as_uint(v), false, false); return __uint_as_float(t[0]) + __uint_as_float(t[1]); }
DEV float swap32add(float v) { auto t = __builtin_amdgcn_permlane32_swap(__float_as_uint(v), __float_as_uint(v), false, false); return __uint_as_float(t[0]) + __uint_as_float(t[1]); }
DEV float red32(float v) { v = dppadd<0xB1>(v); v = dppadd<0x4E>(v); v = dppadd<0x141>(v); v = dppadd<0x140>(v); return swap16add(v); }
DEV f2 cmul(f2 a, f2 b) { f2 s = {-a.y, a.x}; f2 br = {b.x, b.x}, bi = {b.y, b.y}; return a * br + s * bi; }
DEV f2 cexpm(float lr, float ph, float m) {
    float mag = __builtin_amdgcn_exp2f(lr * m), th = __builtin_amdgcn_fractf(ph * m);
    f2 z = {mag * __builtin_amdgcn_cosf(th), mag * __builtin_amdgcn_sinf(th)};
    return z;
}
DEV void lds_barrier() { asm volatile("s_waitcnt lgkmcnt(0)\n\ts_barrier" ::: "memory"); }
DEV void glds(const void* base, u32 voff, u32 dst) {
    asm volatile("s_mov_b32 m0, %2\n\ts_nop 0\n\tglobal_load_lds_dwordx4 %0, %1" ::"v"(voff), "s"(base), "s"(dst) : "memory");
}
DEV v2u trread(u32 addr) { return __builtin_bit_cast(v2u, __builtin_amdgcn_ds_read_tr16_b64_v4i16((LAS s4*)addr)); }
template <typename T> DEV T ldsr(u32 addr) { return *(LAS T*)addr; }
template <typename T> DEV void ldsw(u32 addr, T v) { *(LAS T*)addr = v; }

#define STAMP(i)

__global__ void __launch_bounds__(256, 1)
fused_kernel(const u16* __restrict__ x, const u16* __restrict__ Are, const u16* __restrict__ Aim, const u16* __restrict__ Bre,
             const u16* __restrict__ Bim, const u16* __restrict__ Cre, const u16* __restrict__ Cim, const u16* __restrict__ logdt,
             const u16* __restrict__ Dskip, const u16* __restrict__ w1, const u16* __restrict__ b1, const u16* __restrict__ w2,
             const u16* __restrict__ b2, u16* __restrict__ out, char* ws) {
    __shared__ __attribute__((aligned(16))) char smem[LDS_TOTAL];
    const u32 lb = (u32)(uintptr_t)(LAS char*)smem;
    const int b = blockIdx.x, tid = threadIdx.x, lane = tid & 63, w = __builtin_amdgcn_readfirstlane(tid >> 6);
    const int r = lane & 15, g = lane >> 4;
    const int mt = 2 * (b & 7) + ((b >> 3) & 1), nt = b >> 4;
    const __amdgpu_buffer_rsrc_t rs = __builtin_amdgcn_make_buffer_rsrc(ws, 0, WS_TOTAL, 0x00020000);
    STAMP(0);
    {
        const v4u ones = {~0u, ~0u, ~0u, ~0u};
        __builtin_amdgcn_raw_buffer_store_b128(ones, rs, WS_HP + b * 8192 + w * 2048 + lane * 16, 0, SC1);
        __builtin_amdgcn_raw_buffer_store_b128(ones, rs, WS_HP + b * 8192 + w * 2048 + 1024 + lane * 16, 0, SC1);
    }
    asm volatile("" ::: "memory");
    const u32 ep = __builtin_amdgcn_raw_buffer_load_b32(rs, WS_EPOCH, 0, SC1);
    const int srow = 8 * b + 2 * w + (lane >> 5);
    const v4u xr0 = ((const v4u*)x)[srow * 64 + (lane & 31)], xr1 = ((const v4u*)x)[srow * 64 + 32 + (lane & 31)];
    const int trow = 64 * (b >> 3) + 16 * w + 2 * (lane & 7), tpair = 32 * (b & 7) + 4 * (lane >> 3);
    const v4u xt0 = ((const v4u*)x)[trow * 64 + (tpair >> 2)], xt1 = ((const v4u*)x)[trow * 64 + 64 + (tpair >> 2)];
    const int ch = w >> 1, pidx = (2 * b) * NS + tid;
    const u32 pAr = Are[pidx], pAi = Aim[pidx], pBr = Bre[pidx], pBi = Bim[pidx], pCr = Cre[pidx], pCi = Cim[pidx];
    const u32 pdt = logdt[2 * b + ch];
    const u32 dsk = ((const u32*)Dskip)[b];
    if (tid < 60) { const v4u z = {0, 0, 0, 0}; ldsw<v4u>(lb + LD_UIMG + (tid >= 30 ? UIMG_SZ : 0) + (tid % 30) * 16, z); }
    else if (tid >= 64 && tid < 72) { const v4u z = {0, 0, 0, 0}; ldsw<v4u>(lb + LD_KIMG + ((tid - 64) >> 1) * KIMG_SZ + 4096 + (tid & 1) * 16, z); }
    const u32 tag = __builtin_amdgcn_readfirstlane(ep) + 1u;
    {
        float v[16];
#pragma unroll
        for (int i = 0; i < 4; i++) { v[2 * i] = blo(xr0[i]); v[2 * i + 1] = bhi(xr0[i]); v[8 + 2 * i] = blo(xr1[i]); v[9 + 2 * i] = bhi(xr1[i]); }
        float s = 0;
#pragma unroll
        for (int i = 0; i < 16; i++) s += v[i];
        const float mean = red32(s) * (1.0f / D);
        float q = 0;
#pragma unroll
        for (int i = 0; i < 16; i++) { float dv = v[i] - mean; q += dv * dv; }
        const float var = red32(q) * (1.0f / D);
        const float den = __builtin_amdgcn_sqrtf(rb(rb(var) + 1e-5f));
        const v2u gr = {pk2(mean, den), tag};
        if ((lane & 31) == 0) __builtin_amdgcn_raw_buffer_store_b64(gr, rs, WS_STATS + srow * 8, 0, SC1);
#pragma unroll
        for (int i = 0; i < 4; i++) {
            const v4u o = {xt0[i], tag, xt1[i], tag};
            __builtin_amdgcn_raw_buffer_store_b128(o, rs, WS_XG + ((tpair + i) * L + trow) * 8, 0, SC1);
        }
    }
    STAMP(1);
    {
        const int n = tid & 127;
        const float dt = rb(expf(blo(pdt)));
        const float lr = dt * blo(pAr) * 1.44269504088896341f, ph = dt * blo(pAi) * 0.15915494309189535f;
        f2 cc = {blo(pCr), blo(pCi)}, db = {dt * blo(pBr), dt * blo(pBi)};
        const u32 tb = lb + LD_BIG + ch * T_CH + ((n >> 5) * T_S2 + ((n >> 2) & 7) * T_S1) * 16 + (n & 3) * 4;
        const f2 E1 = cexpm(lr, ph, 1.f), E8 = cexpm(lr, ph, 8.f), E16 = cexpm(lr, ph, 16.f), E256 = cexpm(lr, ph, 256.f);
        f2 ya = {1.f, 0.f}, yb = E8;
#pragma unroll
        for (int j = 0; j < 16; j++) {
            ldsw<u32>(tb + (2 * j) * 16, pk2(ya.x, ya.y));
            ldsw<u32>(tb + (2 * j + 1) * 16, pk2(yb.x, yb.y));
            if (j < 15) { ya = (j == 0) ? E16 : cmul(ya, E16); yb = cmul(yb, E16); }
        }
        f2 X[21];
        X[0] = cmul(cc, db);
#pragma unroll
        for (int e = 1; e < 8; e++) X[e] = cmul(X[e - 1], E1);
#pragma unroll
        for (int e = 0; e < 4; e++) X[8 + e] = cmul(X[2 * e], E256);
        X[12] = cmul(X[8], E256); X[13] = cmul(X[10], E256); X[14] = cmul(X[12], E256); X[15] = cmul(X[13], E256);
        X[16] = cmul(X[14], E256);
#pragma unroll
        for (int e = 17; e < 21; e++) X[e] = cmul(X[e - 1], E256);
#pragma unroll
        for (int e = 0; e < 21; e++) ldsw<u32>(tb + (32 + e) * 16, pk2(X[e].x, -X[e].y));
        lds_barrier();
        const int jt = w & 1;
        const u32 fb = lb + LD_BIG + ch * T_CH + (g * T_S2 + r) * 16;
        f4 k0 = {0, 0, 0, 0}, k1 = {0, 0, 0, 0};
#pragma unroll
        for (int ks = 0; ks < 8; ks++) {
            const v4u yf = ldsr<v4u>(fb + (ks * T_S1 + 16 * jt) * 16), xa = ldsr<v4u>(fb + (ks * T_S1 + 32) * 16), xb = ldsr<v4u>(fb + (ks * T_S1 + 48) * 16);
            k0 = mfma(xa, yf, k0); k1 = mfma(xb, yf, k1);
        }
        const int j = 16 * jt + r;
        const u32 kt = lb + LD_KT + ch * 2048;
        const u32 a0 = g < 2 ? (8 * j + 4 * g) : (g == 2 ? 256 + 4 * j : 384 + 2 * j), a1 = g < 3 ? a0 + 2 : a0 + 64;
        ldsw<u32>(kt + a0 * 2, pk2(k0[0], k0[1]));
        ldsw<u32>(kt + a1 * 2, pk2(k0[2], k0[3]));
#pragma unroll
        for (int i = 0; i < 4; i++) ldsw<u16>(kt + (512 + 32 * (4 * g + i) + j) * 2, (u16)pk2(k1[i], k1[i]));
        lds_barrier();
#pragma unroll
        for (int c2 = 0; c2 < 2; c2++) {
            const int m = n + 128 * c2;
            u32 v[9];
#pragma unroll
            for (int e = 0; e < 9; e++) {
                const int s = 2047 - 8 * m - e;
                const u32 idx = s < 128 ? (u32)max(s, 0) : (pk2((float)s, 0.f) - 0x4300u + 128u);
                v[e] = ldsr<u16>(kt + idx * 2);
            }
            if (m == 255) v[8] = 0;
            const v4u c1 = {v[0] | (v[1] << 16), v[2] | (v[3] << 16), v[4] | (v[5] << 16), v[6] | (v[7] << 16)};
            const v4u c0 = {v[1] | (v[2] << 16), v[3] | (v[4] << 16), v[5] | (v[6] << 16), v[7] | (v[8] << 16)};
            ldsw<v4u>(lb + LD_KIMG + (2 * ch) * KIMG_SZ + m * 16, c0);
            ldsw<v4u>(lb + LD_KIMG + (2 * ch + 1) * KIMG_SZ + m * 16, c1);
        }
    }
    STAMP(2);
    u32 xp[8], up[8];
    {
        v4u gx[4], gs[4];
        const u32 tok0 = 512 * w + 16 * r + 4 * g;
        for (u32 spins = 0;; ++spins) {
            asm volatile("" ::: "memory");
#pragma unroll
            for (int i = 0; i < 4; i++) {
                gs[i] = __builtin_amdgcn_raw_buffer_load_b128(rs, tok0 * 8 + (i >> 1) * 2048 + (i & 1) * 16, WS_STATS, SC1);
                gx[i] = __builtin_amdgcn_raw_buffer_load_b128(rs, tok0 * 8 + (i >> 1) * 2048 + (i & 1) * 16, WS_XG + b * (L * 8), SC1);
            }
            u32 bad = 0;
#pragma unroll
            for (int i = 0; i < 4; i++) bad |= (gs[i][1] ^ tag) | (gs[i][3] ^ tag) | (gx[i][1] ^ tag) | (gx[i][3] ^ tag);
            if (__all(bad == 0) || spins > SPIN_CAP) break;
        }
        STAMP(3);
#pragma unroll
        for (int i = 0; i < 8; i++) {
            const u32 xv = gx[i >> 1][2 * (i & 1)], sv = gs[i >> 1][2 * (i & 1)];
            const float m = blo(sv), ri = __builtin_amdgcn_rcpf(bhi(sv));
            const u32 t = pk2(blo(xv) - m, bhi(xv) - m);
            xp[i] = xv;
            up[i] = pk2(blo(t) * ri, bhi(t) * ri);
        }
#pragma unroll
        for (int e = 0; e < 2; e++) {
            const u32 ua = lb + LD_UIMG + (240 + tok0 + 256 * e) * 2;
            const v2u c0 = {__builtin_amdgcn_perm(up[4 * e + 1], up[4 * e], 0x05040100u), __builtin_amdgcn_perm(up[4 * e + 3], up[4 * e + 2], 0x05040100u)};
            const v2u c1 = {__builtin_amdgcn_perm(up[4 * e + 1], up[4 * e], 0x07060302u), __builtin_amdgcn_perm(up[4 * e + 3], up[4 * e + 2], 0x07060302u)};
            ldsw<v2u>(ua, c0);
            ldsw<v2u>(ua + UIMG_SZ, c1);
        }
    }
    lds_barrier();
    if (b == 0 && tid == 0) __builtin_amdgcn_raw_buffer_store_b32(tag, rs, WS_EPOCH, 0, SC1);
    {
        u32 vo[7];
#pragma unroll
        for (int i = 0; i < 7; i++) { const u32 o = 1024 * i + 16 * lane; vo[i] = (o / 224) * (F * 2) + (o % 224); }
        const char* src = (const char*)w1 + (size_t)(128 * w) * (F * 2) + nt * 224;
#pragma unroll
        for (int q = 0; q < 4; q++)
#pragma unroll
            for (int i = 0; i < 7; i++) glds(src + q * 32 * (F * 2), vo[i], lb + LD_BIG + w * 28672 + q * 7168 + i * 1024);
    }
    STAMP(4);
    {
        f4 acc[8];
#pragma unroll
        for (int c = 0; c < 8; c++) acc[c] = (f4){0, 0, 0, 0};
        const int qh = w & 1;
        const u32 ab = lb + LD_KIMG + (2 * ch + (r & 1)) * KIMG_SZ + (2046 - 16 - 2 * (r >> 1) + 8 * g - 128 * qh) * 2;
        const u32 bb = lb + LD_UIMG + ch * UIMG_SZ + (240 + 16 * r - 16 + 8 * g - 128 * qh) * 2;
#pragma unroll
        for (int q = 0; q < 4; q++) {
            v4u B[8];
#pragma unroll
            for (int c = 0; c < 8; c++) B[c] = ldsr<v4u>(bb + (256 * c - 32 * q) * 2);
#pragma unroll
            for (int p = 0; p < 8; p++) {
                const u32 a = ab - (32 * q + 256 * p) * 2;
                const v4u A = {ldsr<u32>(a), ldsr<u32>(a + 4), ldsr<u32>(a + 8), ldsr<u32>(a + 12)};
#pragma unroll
                for (int c = p; c < 8; c++) acc[c] = mfma(A, B[c - p], acc[c]);
            }
        }
        lds_barrier();
#pragma unroll
        for (int c = 0; c < 8; c++) ldsw<f4>(lb + LD_EX + (w * 8 + c) * 1024 + lane * 16, acc[c]);
        lds_barrier();
        const float d0 = blo(dsk), d1 = bhi(dsk);
#pragma unroll
        for (int e = 0; e < 2; e++) {
            const u32 a = lb + LD_EX + (2 * w + e) * 1024 + lane * 16;
            const f4 y0 = ldsr<f4>(a) + ldsr<f4>(a + 8192), y1 = ldsr<f4>(a + 16384) + ldsr<f4>(a + 24576);
            v4u o;
#pragma unroll
            for (int i = 0; i < 4; i++) {
                const u32 yv = pk2(y0[i], y1[i]), ud = pk2(blo(up[4 * e + i]) * d0, bhi(up[4 * e + i]) * d1);
                const u32 y2 = pk2(blo(yv) + blo(ud), bhi(yv) + bhi(ud));
                o[i] = min(pk2(blo(xp[4 * e + i]) + blo(y2), bhi(xp[4 * e + i]) + bhi(y2)), 0xFFFFFFFEu);
            }
            __builtin_amdgcn_raw_buffer_store_b128(o, rs, WS_HP + b * 8192 + (512 * w + 256 * e + 16 * r + 4 * g) * 4, 0, SC1);
        }
    }
    STAMP(5);
    const u32 wtr = lb + LD_BIG + (4 * g + (r >> 2)) * 224 + (r & 3) * 8;
    u32 b1v[4];
#pragma unroll
    for (int i = 0; i < 4; i++) b1v[i] = (4 * g + i < 7) ? b1[112 * nt + 16 * (4 * g + i) + r] : 0;
    asm volatile("s_waitcnt vmcnt(0)" ::: "memory");
    {
        f4 cs = {0, 0, 0, 0};
#pragma unroll
        for (int k4 = 0; k4 < 4; k4++)
#pragma unroll
            for (int n = 0; n < 7; n++) {
                const u32 a = wtr + (4 * w + k4) * 7168 + n * 32;
                const v2u lo = trread(a), hi = trread(a + 3584);
                const u32 one = (r == n) ? 0x3F803F80u : 0u;
                cs = mfma((v4u){one, one, one, one}, (v4u){lo[0], lo[1], hi[0], hi[1]}, cs);
            }
        ldsw<f4>(lb + LD_CS + w * 1024 + lane * 16, cs);
    }
    STAMP(6);
    v4u raw[32];
    {
        const u32 vo = (2 * g + (r >> 3)) * 8192 + (128 * mt + 32 * w + 4 * (r & 7)) * 4;
        for (u32 spins = 0;; ++spins) {
            asm volatile("" ::: "memory");
#pragma unroll
            for (int i = 0; i < 32; i++) raw[i] = __builtin_amdgcn_raw_buffer_load_b128(rs, vo, WS_HP + (i * 8) * 8192, SC1);
            u32 mx = 0;
#pragma unroll
            for (int i = 0; i < 32; i++) mx = max(max(mx, max(raw[i][0], raw[i][1])), max(raw[i][2], raw[i][3]));
            if (__all(mx != 0xFFFFFFFFu) || spins > SPIN_CAP) break;
        }
    }
    lds_barrier();
    STAMP(7);
    {
        f4 acc[7][2], sq[2], sm[2];
#pragma unroll
        for (int n = 0; n < 7; n++) { acc[n][0] = (f4){0, 0, 0, 0}; acc[n][1] = (f4){0, 0, 0, 0}; }
        sq[0] = sq[1] = sm[0] = sm[1] = (f4){0, 0, 0, 0};
        const v4u ones = {0x3F803F80u, 0x3F803F80u, 0x3F803F80u, 0x3F803F80u};
#pragma unroll
        for (int ks = 0; ks < 16; ks++) {
            v4u fr[2];
#pragma unroll
            for (int e = 0; e < 2; e++)
#pragma unroll
                for (int hf = 0; hf < 2; hf++) {
                    const v4u t = raw[2 * ks + hf];
                    fr[e][2 * hf] = __builtin_amdgcn_update_dpp(t[e], t[2 + e], 0x128, 0xf, 0xc, false);
                    fr[e][2 * hf + 1] = __builtin_amdgcn_update_dpp(t[2 + e], t[e], 0x128, 0xf, 0x3, false);
                }
#pragma unroll
            for (int e = 0; e < 2; e++) { sq[e] = mfma(fr[e], fr[e], sq[e]); sm[e] = mfma(ones, fr[e], sm[e]); }
#pragma unroll
            for (int n = 0; n < 7; n++) {
                const u32 a = wtr + ks * 7168 + n * 32;
                const v2u lo = trread(a), hi = trread(a + 3584);
                const v4u wf = {lo[0], lo[1], hi[0], hi[1]};
                acc[n][0] = mfma(wf, fr[0], acc[n][0]);
                acc[n][1] = mfma(wf, fr[1], acc[n][1]);
            }
        }
        f4 cs = ldsr<f4>(lb + LD_CS + lane * 16) + ldsr<f4>(lb + LD_CS + 1024 + lane * 16) + ldsr<f4>(lb + LD_CS + 2048 + lane * 16) + ldsr<f4>(lb + LD_CS + 3072 + lane * 16);
        v4u st[2];
#pragma unroll
        for (int e = 0; e < 2; e++) {
            float q = 0;
#pragma unroll
            for (int i = 0; i < 4; i++) q += (4 * g + i == r) ? sq[e][i] : 0.f;
            q = swap32add(swap16add(q));
            const float mean = sm[e][0] * (1.0f / D);
            const float var = fmaxf(q * (1.0f / D) - mean * mean, 0.f);
            const float den = rb(__builtin_amdgcn_sqrtf(rb(rb(var) + 1e-5f)));
            st[e] = (v4u){pk2(-mean, -mean), pk2(den, 0.f), 0u, 0u};
            const float ri = __builtin_amdgcn_rcpf(den);
            if (g == 0) ldsw<float>(lb + LD_RI + ((2 * w + e) * 16 + r) * 4, ri * ri);
        }
#pragma unroll
        for (int n = 0; n < 7; n++) {
            const float c = cs[n & 3], chi = rb(c);
            const bool on = (g == (n >> 2));
            const v4u af = {on ? pk2(chi, c - chi) : 0u, on ? b1v[n & 3] : 0u, 0u, 0u};
            acc[n][0] = mfma(af, st[0], acc[n][0]);
            acc[n][1] = mfma(af, st[1], acc[n][1]);
        }
#pragma unroll
        for (int e = 0; e < 2; e++) {
            u32 pk[14];
#pragma unroll
            for (int n = 0; n < 7; n++)
#pragma unroll
                for (int i = 0; i < 2; i++) {
                    const float u0 = acc[n][e][2 * i], u1 = acc[n][e][2 * i + 1];
                    const s2 t = __builtin_bit_cast(s2, pk2(u0 * fabsf(u0), u1 * fabsf(u1)));
                    pk[2 * n + i] = __builtin_bit_cast(u32, __builtin_elementwise_max(t, (s2){0, 0}));
                }
            const u32 base = WS_A2 + ((8 * mt + 2 * w + e) * 56 + 7 * (nt >> 1)) * 1024;
#pragma unroll
            for (int m = 0; m < 3; m++)
                __builtin_amdgcn_raw_buffer_store_b128((v4u){pk[4 * m], pk[4 * m + 1], pk[4 * m + 2], pk[4 * m + 3]}, rs, lane * 16, base + (3 * (nt & 1) + m) * 1024, SC1);
            __builtin_amdgcn_raw_buffer_store_b64((v2u){pk[12], pk[13]}, rs, lane * 8, base + 6 * 1024 + (nt & 1) * 512, SC1);
        }
    }
    STAMP(8);
    asm volatile("s_waitcnt vmcnt(0)" ::: "memory");
    if (lane == 0) __builtin_amdgcn_raw_buffer_store_b32(tag, rs, WS_FLAG + ((mt * 2 + (w >> 1)) * 32 + nt * 2 + (w & 1)) * 4, 0, SC1);
    lds_barrier();
    STAMP(9);
    const int half = nt & 1, dtile = nt >> 1;
    v4u wf2[14][4];
    {
        const u32 row = lane >> 3, chunk = (lane & 7) ^ (((row >> 1) & 3) << 1);
        const u32 vo = row * (D * 2) + chunk * 16;
        const char* src = (const char*)w2 + (size_t)(448 * w) * (D * 2) + dtile * 128;
        const u32 rrow = 4 * g + (r >> 2);
        const u32 tb = lb + LD_BIG + w * 28672 + rrow * 128 + (r & 1) * 8;
#pragma unroll
        for (int hf = 0; hf < 2; hf++) {
#pragma unroll
            for (int i = 0; i < 28; i++) glds(src + (224 * hf + 8 * i) * (D * 2), vo, lb + LD_BIG + w * 28672 + i * 1024);
            asm volatile("s_waitcnt vmcnt(0)" ::: "memory");
#pragma unroll
            for (int kk = 0; kk < 7; kk++) {
                const int r0 = kk < 3 ? 32 * kk : (kk < 6 ? 112 + 32 * (kk - 3) : 96), r1 = kk < 6 ? r0 + 16 : 208;
#pragma unroll
                for (int i = 0; i < 4; i++) {
                    const u32 co = (((2 * i + ((r >> 1) & 1)) ^ (((rrow >> 1) & 3) << 1)) * 16);
                    const v2u lo = trread(tb + r0 * 128 + co), hi = trread(tb + r1 * 128 + co);
                    wf2[7 * hf + kk][i] = (v4u){lo[0], lo[1], hi[0], hi[1]};
                }
            }
            asm volatile("s_waitcnt lgkmcnt(0)" ::: "memory");
        }
    }
    const int tokf = 128 * mt + 64 * half + 32 * (w >> 1) + 4 * (r & 7) + 2 * (r >> 3) + (w & 1);
    u32 hres[8];
    v2u b2v[4];
#pragma unroll
    for (int i = 0; i < 8; i++) hres[i] = __builtin_amdgcn_raw_buffer_load_b32(rs, (2 * g) * 8192 + tokf * 4, WS_HP + (32 * dtile + 8 * (i >> 1) + (i & 1)) * 8192, SC1);
#pragma unroll
    for (int i = 0; i < 4; i++) b2v[i] = *(const v2u*)(b2 + 64 * dtile + 16 * i + 4 * g);
    STAMP(10);
    {
        const u32 fo = WS_FLAG + ((mt * 2 + half) * 32 + 8 * w + (lane & 7)) * 4;
        for (u32 spins = 0;; ++spins) {
            asm volatile("" ::: "memory");
            const u32 v = __builtin_amdgcn_raw_buffer_load_b32(rs, fo, 0, SC1);
            if (__all(v == tag) || spins > SPIN_CAP) break;
        }
        asm volatile("" ::: "memory");
    }
    STAMP(11);
    {
        lds_barrier();
#pragma unroll
        for (int t = 0; t < 4; t++) {
            const u32 base = WS_A2 + ((8 * mt + 4 * half + t) * 56 + 14 * w) * 1024;
            v4u af[14];
#pragma unroll
            for (int kk = 0; kk < 14; kk++) {
                if (kk % 7 < 6) af[kk] = __builtin_amdgcn_raw_buffer_load_b128(rs, lane * 16, base + kk * 1024, SC1);
                else {
                    const v2u lo = __builtin_amdgcn_raw_buffer_load_b64(rs, lane * 8, base + kk * 1024, SC1), hi = __builtin_amdgcn_raw_buffer_load_b64(rs, lane * 8, base + kk * 1024 + 512, SC1);
                    af[kk] = (v4u){lo[0], lo[1], hi[0], hi[1]};
                }
            }
            f4 acc[4];
#pragma unroll
            for (int i = 0; i < 4; i++) acc[i] = (f4){0, 0, 0, 0};
#pragma unroll
            for (int kk = 0; kk < 14; kk++)
#pragma unroll
                for (int i = 0; i < 4; i++) acc[i] = mfma(wf2[kk][i], af[kk], acc[i]);
#pragma unroll
            for (int i = 0; i < 4; i++) ldsw<f4>(lb + LD_BIG + ((t * 4 + w) * 4 + i) * 1024 + lane * 16, acc[i]);
        }
        lds_barrier();
        STAMP(12);
        const float ri2 = ldsr<float>(lb + LD_RI + ((4 * half + w) * 16 + r) * 4);
#pragma unroll
        for (int i = 0; i < 4; i++) {
            const u32 a = lb + LD_BIG + (w * 16 + i) * 1024 + lane * 16;
            const f4 s = (ldsr<f4>(a) + ldsr<f4>(a + 4096) + ldsr<f4>(a + 8192) + ldsr<f4>(a + 12288)) * ri2;
            v2u o;
#pragma unroll
            for (int k = 0; k < 2; k++) {
                const u32 t0 = pk2(s[2 * k], s[2 * k + 1]);
                const u32 t1 = pk2(blo(t0) + blo(b2v[i][k]), bhi(t0) + bhi(b2v[i][k]));
                o[k] = pk2(blo(hres[2 * i + k]) + blo(t1), bhi(hres[2 * i + k]) + bhi(t1));
            }
            *(v2u*)(out + (size_t)tokf * D + 64 * dtile + 16 * i + 4 * g) = o;
        }
    }
    STAMP(13);
}

__global__ void k0(char* ws) { if (threadIdx.x == 0) *(volatile unsigned long long*)(ws + WS_PROF) = __builtin_amdgcn_s_memrealtime(); }
__global__ void k2(char* ws) { unsigned long long t0 = *(volatile unsigned long long*)(ws + WS_PROF); for (int i = 0; i < 10000000 && __builtin_amdgcn_s_memrealtime() < t0 + 8000; i++) __builtin_amdgcn_s_sleep(1); }
extern "C" void kernel_launch(void* const* d_in, const int* in_sizes, int n_in, void* d_out, int out_size, void* d_ws, size_t ws_size,
                              hipStream_t stream) {
    k0<<<1, 64, 0, stream>>>((char*)d_ws);
    fused_kernel<<<256, 256, 0, stream>>>((const u16*)d_in[0], (const u16*)d_in[1], (const u16*)d_in[2], (const u16*)d_in[3], (const u16*)d_in[4],
                                          (const u16*)d_in[5], (const u16*)d_in[6], (const u16*)d_in[7], (const u16*)d_in[8], (const u16*)d_in[10],
                                          (const u16*)d_in[11], (const u16*)d_in[12], (const u16*)d_in[13], (u16*)d_out, (char*)d_ws);
    k2<<<1, 64, 0, stream>>>((char*)d_ws);
}
```

```cpp
#include <hip/hip_runtime.h>
#include <stdint.h>

typedef unsigned short u16;
static constexpr unsigned WS_HP = 4194304u, WS_PROF = 15728640u;

__global__ void __launch_bounds__(256, 1) __attribute__((aligned(4096)))
fused_kernel(const u16* x, const u16* Are, const u16* Aim, const u16* Bre, const u16* Bim, const u16* Cre, const u16* Cim,
             const u16* logdt, const u16* Dskip, const u16* w1, const u16* b1, const u16* w2, const u16* b2, u16* out, char* ws) {
#if defined(__HIP_DEVICE_COMPILE__)
    __shared__ __attribute__((aligned(16))) char smem[163840];
    asm volatile(
        "s_load_dwordx16 s[8:23], s[0:1], 0\n"
        "s_load_dwordx2 s[36:37], s[0:1], 0x70\n"
        "s_load_dwordx8 s[24:31], s[0:1], 64\n"
        "s_load_dwordx4 s[32:35], s[0:1], 0x60\n"
        "s_getpc_b64 s[52:53]\n"
        "s_lshr_b32 s5, s2, 3\n"
        "s_lshl_b32 s49, s5, 10\n"
        "v_lshrrev_b32 v28, 5, v0\n"
        "v_lshl_add_u32 v28, v28, 7, s49\n"
        "v_min_u32 v28, 0x5f00, v28\n"
        "global_load_dword v65, v28, s[52:53]\n"
        "v_lshrrev_b32 v24, 6, v0\n"
        "v_and_b32 v1, 63, v0\n"
        "v_readfirstlane_b32 s40, v24\n"
        "v_and_b32 v2, 15, v0\n"
        "v_lshrrev_b32 v3, 4, v1\n"
        "v_lshlrev_b32 v4, 4, v1\n"
        "s_mov_b32 s38, 0x1000000\n"
        "s_mov_b32 s39, 0x20000\n"
        "s_mov_b32 s58, 0\n"
        "s_and_b32 s6, s2, 7\n"
        "s_lshr_b32 s42, s40, 1\n"
        "s_lshr_b32 s50, s5, 3\n"
        "s_lshl2_add_u32 s50, s6, s50\n"
        "s_lshl2_add_u32 s50, s50, s40\n"
        "s_and_b32 s51, s5, 7\n"
        "s_lshl_b32 s49, s50, 14\n"
        "s_lshl_b32 s59, s51, 7\n"
        "s_add_u32 s49, s49, s59\n"
        "v_and_b32 v24, 7, v1\n"
        "v_lshrrev_b32 v25, 3, v1\n"
        "v_lshlrev_b32 v26, 11, v24\n"
        "v_lshl_or_b32 v26, v25, 4, v26\n"
        "v_add_u32 v73, s49, v26\n"
        "s_lshl_b32 s49, s51, 19\n"
        "s_lshl_b32 s59, s50, 7\n"
        "s_add_u32 s49, s49, s59\n"
        "v_lshlrev_b32 v26, 16, v25\n"
        "v_lshl_or_b32 v26, v24, 4, v26\n"
        "v_add_u32 v75, s49, v26\n"
        "s_lshl_b32 s49, s6, 5\n"
        "s_add_u32 s49, s49, s5\n"
        "s_lshl_b32 s50, s49, 6\n"
        "v_add_u32 v44, s50, v4\n"
        "s_lshl2_add_u32 s49, s49, s40\n"
        "s_lshl_b32 s49, s49, 11\n"
        "v_lshrrev_b32 v24, 5, v1\n"
        "v_and_b32 v25, 31, v1\n"
        "v_lshl_add_u32 v26, v24, 10, s49\n"
        "v_lshl_add_u32 v72, v25, 4, v26\n"
        "s_lshl_b32 s50, s40, 4\n"
        "s_add_u32 s50, s50, 0x27600\n"
        "v_lshl_add_u32 v45, v24, 3, s50\n"
        "s_lshl_b32 s49, s2, 9\n"
        "v_lshl_add_u32 v74, v0, 1, s49\n"
        "v_mov_b64 v[66:67], 0\n"
        "v_mov_b64 v[68:69], 0\n"
        "v_mov_b64 v[70:71], 0\n"
        "v_mov_b64 v[32:33], 0\n"
        "v_mov_b64 v[34:35], 0\n"
        "s_sub_u32 s49, s2, 0xe0\n"
        "s_min_u32 s59, s2, s49\n"
        "s_mul_i32 s49, s59, 0x124a\n"
        "s_lshr_b32 s49, s49, 16\n"
        "s_mul_i32 s50, s49, 14\n"
        "s_sub_u32 s50, s59, s50\n"
        "s_and_b32 s59, s49, 3\n"
        "s_mul_i32 s51, s59, 0x70000\n"
        "s_lshr_b32 s49, s49, 2\n"
        "s_lshl1_add_u32 s63, s49, s42\n"
        "s_lshl2_add_u32 s63, s63, s59\n"
        "s_mul_i32 s63, s63, 14\n"
        "s_add_u32 s63, s63, s50\n"
        "s_and_b32 s59, s40, 1\n"
        "s_lshl1_add_u32 s63, s63, s59\n"
        "s_lshl2_add_u32 s59, s2, s40\n"
        "s_cmp_lt_u32 s2, 0xe0\n"
        "s_cselect_b32 s63, s63, s59\n"
        "s_lshl2_add_u32 s49, s49, s40\n"
        "s_lshl_b32 s49, s49, 6\n"
        "s_add_u32 s51, s51, s49\n"
        "s_sub_u32 s49, s50, 2\n"
        "s_cmp_lt_u32 s50, 4\n"
        "s_cselect_b32 s49, s50, s49\n"
        "s_and_b32 s59, s49, 3\n"
        "s_mul_i32 s59, s59, 0x70\n"
        "s_lshr_b32 s49, s49, 2\n"
        "s_lshl_b32 s49, s49, 5\n"
        "s_add_u32 s59, s59, s49\n"
        "s_mul_i32 s49, s50, 0xe0\n"
        "s_sub_u32 s49, s49, 0x320\n"
        "s_lshr_b32 s50, s50, 1\n"
        "s_cmp_eq_u32 s50, 2\n"
        "s_cselect_b32 s59, s49, s59\n"
        "s_cselect_b32 s50, 0x70, 16\n"
        "s_lshl_b32 s50, s50, 10\n"
        "s_lshl_b32 s59, s59, 10\n"
        "s_add_u32 s51, s51, s59\n"
        "v_lshrrev_b32 v24, 2, v1\n"
        "v_and_b32 v25, 3, v1\n"
        "v_lshl_add_u32 v26, v24, 10, s51\n"
        "v_lshl_add_u32 v41, v25, 4, v26\n"
        "v_add_u32 v42, s50, v41\n"
        "v_lshlrev_b32 v26, 8, v24\n"
        "v_lshl_add_u32 v5, v25, 3, v26\n"
        "v_and_b32 v6, 7, v24\n"
        "s_lshl_b32 s49, s40, 11\n"
        "s_add_u32 s49, s49, 0x10000\n"
        "s_mov_b32 m0, s49\n"
        "v_lshl_add_u32 v26, v24, 6, s49\n"
        "v_lshl_add_u32 v40, v25, 3, v26\n"
        "s_and_b32 s49, s5, 3\n"
        "s_lshr_b32 s46, s5, 2\n"
        "s_lshr_b32 s50, s6, 1\n"
        "s_lshl2_add_u32 s43, s50, s49\n"
        "s_lshl2_add_u32 s45, s6, s49\n"
        "s_and_b32 s50, s6, 1\n"
        "s_lshl3_add_u32 s44, s50, s46\n"
        "s_lshl1_add_u32 s7, s49, s46\n"
        "s_and_b32 s7, s7, 7\n"
        "s_lshl_b32 s59, s2, 2\n"
        "s_waitcnt lgkmcnt(0)\n"
        "global_load_short_d16_hi v66, v74, s[10:11]\n"
        "global_load_short_d16_hi v67, v74, s[12:13]\n"
        "global_load_short_d16_hi v68, v74, s[14:15]\n"
        "global_load_short_d16_hi v69, v74, s[16:17]\n"
        "global_load_short_d16_hi v70, v74, s[18:19]\n"
        "global_load_short_d16_hi v71, v74, s[20:21]\n"
        "global_load_dwordx4 v[48:51], v72, s[8:9]\n"
        "global_load_dwordx4 v[52:55], v72, s[8:9] offset:512\n"
        "s_and_b32 s37, s37, 0xffff\n"
        "buffer_load_dword v64, v32, s[36:39], 0 offen sc1\n"
        "s_load_dword s47, s[22:23], s59\n"
        "s_load_dword s48, s[24:25], s59\n"
        "s_barrier\n"
        "global_load_dwordx4 v[56:59], v73, s[8:9]\n"
        "global_load_dwordx4 v[60:63], v73, s[8:9] offset:1024\n"
        "s_barrier\n"
        "global_load_lds_dwordx4 v41, s[30:31]\n"
        "s_add_u32 m0, m0, 0x400\n"
        "s_mov_b32 s1, 0xffff0000\n"
        "global_load_lds_dwordx4 v42, s[30:31]\n"
        "v_and_b32 v80, 0x7f, v0\n"
        "s_mul_i32 s49, s42, 0x810\n"
        "s_add_u32 s49, s49, 0x1e010\n"
        "v_lshlrev_b32 v24, 3, v80\n"
        "v_sub_u32 v24, 0x7ff, v24\n"
        "v_cvt_f32_u32 v25, v24\n"
        "s_mov_b32 s50, 0xffff\n"
        "s_mov_b32 s51, 0x43000000\n"
        "v_mov_b32 v28, v25\n"
        "v_add_f32 v27, -1.0, v25\n"
        "v_cvt_pk_bf16_f32 v26, v25, v27\n"
        "v_add_f32 v25, -2.0, v25\n"
        "v_and_b32 v27, s50, v26\n"
        "v_lshl_add_u32 v82, v27, 1, s49\n"
        "v_lshrrev_b32 v27, 16, v26\n"
        "v_lshl_add_u32 v83, v27, 1, s49\n"
        "v_add_f32 v27, -1.0, v25\n"
        "v_cvt_pk_bf16_f32 v26, v25, v27\n"
        "v_add_f32 v25, -2.0, v25\n"
        "v_and_b32 v27, s50, v26\n"
        "v_lshl_add_u32 v84, v27, 1, s49\n"
        "v_lshrrev_b32 v27, 16, v26\n"
        "v_lshl_add_u32 v85, v27, 1, s49\n"
        "v_add_f32 v27, -1.0, v25\n"
        "v_cvt_pk_bf16_f32 v26, v25, v27\n"
        "v_add_f32 v25, -2.0, v25\n"
        "v_and_b32 v27, s50, v26\n"
        "v_lshl_add_u32 v86, v27, 1, s49\n"
        "v_lshrrev_b32 v27, 16, v26\n"
        "v_lshl_add_u32 v87, v27, 1, s49\n"
        "v_add_f32 v27, -1.0, v25\n"
        "v_cvt_pk_bf16_f32 v26, v25, v27\n"
        "v_add_f32 v25, -2.0, v25\n"
        "v_and_b32 v27, s50, v26\n"
        "v_lshl_add_u32 v88, v27, 1, s49\n"
        "v_lshrrev_b32 v27, 16, v26\n"
        "v_lshl_add_u32 v89, v27, 1, s49\n"
        "v_add_f32 v27, -1.0, v25\n"
        "v_cvt_pk_bf16_f32 v26, v25, v27\n"
        "v_and_b32 v27, s50, v26\n"
        "v_lshl_add_u32 v90, v27, 1, s49\n"
        "v_add_f32 v25, 0xc4800000, v28\n"
        "v_add_f32 v27, -1.0, v25\n"
        "v_cvt_pk_bf16_f32 v26, v25, v27\n"
        "v_add_f32 v25, -2.0, v25\n"
        "v_and_b32 v27, s50, v26\n"
        "v_lshl_add_u32 v91, v27, 1, s49\n"
        "v_lshrrev_b32 v27, 16, v26\n"
        "v_lshl_add_u32 v92, v27, 1, s49\n"
        "v_add_f32 v27, -1.0, v25\n"
        "v_cvt_pk_bf16_f32 v26, v25, v27\n"
        "v_add_f32 v25, -2.0, v25\n"
        "v_and_b32 v27, s50, v26\n"
        "v_lshl_add_u32 v93, v27, 1, s49\n"
        "v_lshrrev_b32 v27, 16, v26\n"
        "v_lshl_add_u32 v94, v27, 1, s49\n"
        "v_add_f32 v27, -1.0, v25\n"
        "v_cvt_pk_bf16_f32 v26, v25, v27\n"
        "v_add_f32 v25, -2.0, v25\n"
        "v_and_b32 v27, s50, v26\n"
        "v_lshl_add_u32 v95, v27, 1, s49\n"
        "v_lshrrev_b32 v27, 16, v26\n"
        "v_lshl_add_u32 v96, v27, 1, s49\n"
        "v_add_f32 v27, -1.0, v25\n"
        "v_cvt_pk_bf16_f32 v26, v25, v27\n"
        "v_add_f32 v25, -2.0, v25\n"
        "v_and_b32 v27, s50, v26\n"
        "v_lshl_add_u32 v97, v27, 1, s49\n"
        "v_lshrrev_b32 v27, 16, v26\n"
        "v_lshl_add_u32 v98, v27, 1, s49\n"
        "v_add_f32 v27, -1.0, v25\n"
        "v_cvt_pk_bf16_f32 v26, v25, v27\n"
        "v_and_b32 v27, s50, v26\n"
        "v_lshl_add_u32 v99, v27, 1, s49\n"
        "s_mul_i32 s49, s42, 0x810\n"
        "s_add_u32 s49, s49, 0x25d10\n"
        "v_cmp_le_u32 vcc, 0x60, v80\n"
        "v_lshl_add_u32 v24, v24, 1, s49\n"
        "s_nop 0\n"
        "v_cndmask_b32 v91, v91, v24, vcc\n"
        "v_add_u32 v24, -2, v24\n"
        "v_cndmask_b32 v92, v92, v24, vcc\n"
        "v_add_u32 v24, -2, v24\n"
        "v_cndmask_b32 v93, v93, v24, vcc\n"
        "v_add_u32 v24, -2, v24\n"
        "v_cndmask_b32 v94, v94, v24, vcc\n"
        "v_add_u32 v24, -2, v24\n"
        "v_cndmask_b32 v95, v95, v24, vcc\n"
        "v_add_u32 v24, -2, v24\n"
        "v_cndmask_b32 v96, v96, v24, vcc\n"
        "v_add_u32 v24, -2, v24\n"
        "v_cndmask_b32 v97, v97, v24, vcc\n"
        "v_add_u32 v24, -2, v24\n"
        "v_cndmask_b32 v98, v98, v24, vcc\n"
        "v_add_u32 v24, -2, v24\n"
        "v_cndmask_b32 v99, v99, v24, vcc\n"
        "s_mul_i32 s49, s42, 0x6c00\n"
        "s_add_u32 s49, s49, 0x12000\n"
        "v_lshrrev_b32 v25, 5, v80\n"
        "v_mul_u32_u24 v25, 0x1b0, v25\n"
        "v_bfe_u32 v26, v80, 2, 3\n"
        "v_mad_u32_u24 v25, v26, 53, v25\n"
        "v_and_b32 v26, 3, v80\n"
        "v_lshl_add_u32 v26, v26, 2, s49\n"
        "v_lshl_add_u32 v100, v25, 4, v26\n"
        "v_mul_u32_u24 v25, 0x1b0, v3\n"
        "v_add_u32 v25, v25, v2\n"
        "v_lshl_add_u32 v101, v25, 4, s49\n"
        "s_and_b32 s50, s40, 1\n"
        "s_lshl_b32 s51, s50, 8\n"
        "v_add_u32 v105, s51, v101\n"
        "s_lshl_b32 s50, s50, 4\n"
        "v_add_u32 v25, s50, v2\n"
        "s_mul_i32 s49, s42, 0x810\n"
        "s_add_u32 s49, s49, 0x26510\n"
        "v_cmp_eq_u32 vcc, 2, v3\n"
        "v_lshlrev_b32 v26, 4, v25\n"
        "v_lshl_add_u32 v26, v3, 3, v26\n"
        "v_lshlrev_b32 v27, 3, v25\n"
        "v_add_u32 v27, 0x200, v27\n"
        "v_cndmask_b32 v26, v26, v27, vcc\n"
        "v_cmp_eq_u32 vcc, 3, v3\n"
        "v_lshlrev_b32 v27, 2, v25\n"
        "v_add_u32 v27, 0x300, v27\n"
        "v_cndmask_b32 v26, v26, v27, vcc\n"
        "v_add_u32 v102, s49, v26\n"
        "v_mov_b32 v28, 0x80\n"
        "v_cndmask_b32 v27, 4, v28, vcc\n"
        "v_add_u32 v103, v102, v27\n"
        "v_lshl_add_u32 v26, v3, 8, s49\n"
        "v_lshl_add_u32 v104, v25, 1, v26\n"
        "s_movk_i32 s99, 0x4000\n"
        "s_mul_i32 s50, s44, 0xe0\n"
        "s_and_b32 s50, s50, 0xffffffc0\n"
        "s_add_u32 s26, s26, s50\n"
        "s_addc_u32 s27, s27, 0\n"
        "v_lshlrev_b32 v25, 1, v3\n"
        "v_xor_b32 v25, v25, v2\n"
        "v_mul_u32_u24 v26, 0xe00, v3\n"
        "v_lshl_add_u32 v13, v25, 4, v26\n"
        "v_xor_b32 v14, 0x80, v13\n"
        "v_add_u32 v14, 0x3800, v14\n"
        "s_movk_i32 s50, 0x7000\n"
        "v_add_u32 v15, s50, v13\n"
        "v_add_u32 v16, s50, v14\n"
        "v_add_u32 v17, s50, v15\n"
        "v_add_u32 v18, s50, v16\n"
        "v_add_u32 v19, s50, v17\n"
        "v_add_u32 v20, s50, v18\n"
        "s_lshl1_add_u32 s51, s7, s40\n"
        "s_and_b32 s59, s51, 15\n"
        "s_mul_i32 s59, s59, 0x1c000\n"
        "s_add_u32 s10, s26, s59\n"
        "s_addc_u32 s11, s27, 0\n"
        "s_add_u32 s51, s51, 4\n"
        "s_and_b32 s59, s51, 15\n"
        "s_mul_i32 s59, s59, 0x1c000\n"
        "s_add_u32 s12, s26, s59\n"
        "s_addc_u32 s13, s27, 0\n"
        "s_add_u32 s51, s51, 4\n"
        "s_and_b32 s59, s51, 15\n"
        "s_mul_i32 s59, s59, 0x1c000\n"
        "s_add_u32 s14, s26, s59\n"
        "s_addc_u32 s15, s27, 0\n"
        "s_add_u32 s51, s51, 4\n"
        "s_and_b32 s59, s51, 15\n"
        "s_mul_i32 s59, s59, 0x1c000\n"
        "s_add_u32 s16, s26, s59\n"
        "s_addc_u32 s17, s27, 0\n"
        "s_lshl_b32 s24, s40, 12\n"
        "s_mul_i32 s49, s42, 0x1200\n"
        "s_add_u32 s49, s49, 0x24100\n"
        "v_add_u32 v24, s49, v4\n"
        "s_mul_i32 s49, s42, 0x2080\n"
        "s_add_u32 s49, s49, 0x21000\n"
        "v_cmp_gt_u32 vcc, 30, v1\n"
        "v_lshrrev_b32 v25, 5, v1\n"
        "v_mul_u32_u24 v25, 0x1040, v25\n"
        "v_and_b32 v26, 1, v1\n"
        "v_lshl_add_u32 v25, v26, 4, v25\n"
        "v_add_u32 v25, s49, v25\n"
        "v_cndmask_b32 v24, v25, v24, vcc\n"
        "v_cmp_gt_u32 vcc, 34, v1\n"
        "s_mul_i32 s49, s42, 0x810\n"
        "s_add_u32 s49, s49, 0x26500\n"
        "v_mov_b32 v25, s49\n"
        "v_cndmask_b32 v24, v25, v24, vcc\n"
        "ds_write_b128 v24, v[32:35]\n"
        "s_mul_i32 s49, s42, 0x2080\n"
        "s_add_u32 s49, s49, 0x20000\n"
        "v_lshl_add_u32 v43, v80, 4, s49\n"
        "s_lshl_b32 s50, s40, 12\n"
        "v_add_u32 v8, s50, v4\n"
        "s_and_b32 s50, s40, 1\n"
        "s_lshl_b32 s50, s50, 8\n"
        "s_sub_u32 s51, s49, s50\n"
        "s_add_u32 s51, s51, 0xc00\n"
        "v_and_b32 v24, 1, v2\n"
        "v_mul_u32_u24 v24, 0x1042, v24\n"
        "v_lshlrev_b32 v25, 1, v2\n"
        "v_sub_u32 v24, v24, v25\n"
        "v_lshl_add_u32 v24, v3, 4, v24\n"
        "v_add_u32 v36, s51, v24\n"
        "v_add_u32 v37, 0xfffffc00, v36\n"
        "v_add_u32 v38, 0xfffff800, v36\n"
        "v_add_u32 v39, 0xfffff400, v36\n"
        "s_lshl_b32 s98, s2, 14\n"
        "s_add_u32 s98, s98, 0x400000\n"
        "s_waitcnt lgkmcnt(0)\n"
        "s_lshl_b32 s49, s47, 16\n"
        "s_and_b32 s51, s47, s1\n"
        "s_cmp_eq_u32 s42, 0\n"
        "s_cselect_b32 s49, s49, s51\n"
        "s_lshl_b32 s60, s48, 16\n"
        "s_and_b32 s61, s48, s1\n"
        "v_mov_b32 v24, 0x3fb8aa3b\n"
        "v_mul_f32 v25, s49, v24\n"
        "v_fma_f32 v26, s49, v24, -v25\n"
        "v_mov_b32 v27, 0x32a5705f\n"
        "v_exp_f32 v25, v25\n"
        "v_fma_f32 v26, s49, v27, v26\n"
        "v_mul_f32 v26, 0x3f317218, v26\n"
        "v_fma_f32 v25, v25, v26, v25\n"
        "v_cvt_pk_bf16_f32 v46, v25, v25\n"
        "v_and_b32 v46, s1, v46\n"
        "s_waitcnt vmcnt(12)\n"
        "v_mul_f32 v117, v46, v66\n"
        "s_waitcnt vmcnt(11)\n"
        "v_mul_f32 v118, v46, v67\n"
        "v_mul_f32 v117, 0x3fb8aa3b, v117\n"
        "v_mul_f32 v118, 0.15915494, v118\n"
        "v_mul_f32 v25, 0x41000000, v118\n"
        "v_fract_f32 v29, v118\n"
        "v_mul_f32 v31, 0x43800000, v118\n"
        "v_mul_f32 v24, 0x41000000, v117\n"
        "v_exp_f32 v28, v117\n"
        "v_mul_f32 v30, 0x43800000, v117\n"
        "v_fract_f32 v25, v25\n"
        "v_cos_f32 v120, v29\n"
        "v_fract_f32 v31, v31\n"
        "v_exp_f32 v24, v24\n"
        "v_sin_f32 v121, v29\n"
        "v_exp_f32 v30, v30\n"
        "v_cos_f32 v122, v25\n"
        "v_mul_f32 v120, v28, v120\n"
        "v_cos_f32 v126, v31\n"
        "v_sin_f32 v123, v25\n"
        "v_mul_f32 v121, v28, v121\n"
        "v_sin_f32 v127, v31\n"
        "v_mul_f32 v122, v24, v122\n"
        "v_mul_f32 v126, v30, v126\n"
        "v_mul_f32 v123, v24, v123\n"
        "v_mul_f32 v127, v30, v127\n"
        "v_mov_b32 v154, 0x3f80\n"
        "v_cvt_pk_bf16_f32 v155, v122, v123\n"
        "ds_write2_b32 v100, v154, v155 offset0:0 offset1:4\n"
        "s_add_u32 m0, s24, 0\n"
        "v_pk_mul_f32 v[150:151], v[122:123], v[122:123] op_sel:[0,0] op_sel_hi:[0,1]\n"
        "v_pk_fma_f32 v[148:149], v[122:123], v[122:123], v[150:151] op_sel:[1,1,0] op_sel_hi:[1,0,1] neg_lo:[0,1,0]\n"
        "v_cvt_pk_bf16_f32 v156, v148, v149\n"
        "v_pk_mul_f32 v[150:151], v[148:149], v[122:123] op_sel:[0,0] op_sel_hi:[0,1]\n"
        "v_pk_fma_f32 v[148:149], v[148:149], v[122:123], v[150:151] op_sel:[1,1,0] op_sel_hi:[1,0,1] neg_lo:[0,1,0]\n"
        "global_load_lds_dwordx4 v13, s[10:11]\n"
        "v_cvt_pk_bf16_f32 v157, v148, v149\n"
        "ds_write2_b32 v100, v156, v157 offset0:8 offset1:12\n"
        "v_pk_mul_f32 v[150:151], v[148:149], v[122:123] op_sel:[0,0] op_sel_hi:[0,1]\n"
        "v_pk_fma_f32 v[148:149], v[148:149], v[122:123], v[150:151] op_sel:[1,1,0] op_sel_hi:[1,0,1] neg_lo:[0,1,0]\n"
        "v_cvt_pk_bf16_f32 v154, v148, v149\n"
        "s_add_u32 m0, s24, 0x400\n"
        "v_pk_mul_f32 v[150:151], v[148:149], v[122:123] op_sel:[0,0] op_sel_hi:[0,1]\n"
        "v_pk_fma_f32 v[148:149], v[148:149], v[122:123], v[150:151] op_sel:[1,1,0] op_sel_hi:[1,0,1] neg_lo:[0,1,0]\n"
        "v_cvt_pk_bf16_f32 v155, v148, v149\n"
        "ds_write2_b32 v100, v154, v155 offset0:16 offset1:20\n"
        "v_pk_mul_f32 v[150:151], v[148:149], v[122:123] op_sel:[0,0] op_sel_hi:[0,1]\n"
        "global_load_lds_dwordx4 v14, s[10:11]\n"
        "v_pk_fma_f32 v[148:149], v[148:149], v[122:123], v[150:151] op_sel:[1,1,0] op_sel_hi:[1,0,1] neg_lo:[0,1,0]\n"
        "v_cvt_pk_bf16_f32 v156, v148, v149\n"
        "v_pk_mul_f32 v[150:151], v[148:149], v[122:123] op_sel:[0,0] op_sel_hi:[0,1]\n"
        "v_pk_fma_f32 v[148:149], v[148:149], v[122:123], v[150:151] op_sel:[1,1,0] op_sel_hi:[1,0,1] neg_lo:[0,1,0]\n"
        "v_cvt_pk_bf16_f32 v157, v148, v149\n"
        "s_add_u32 m0, s24, 0x800\n"
        "ds_write2_b32 v100, v156, v157 offset0:24 offset1:28\n"
        "v_pk_mul_f32 v[150:151], v[148:149], v[122:123] op_sel:[0,0] op_sel_hi:[0,1]\n"
        "v_pk_fma_f32 v[148:149], v[148:149], v[122:123], v[150:151] op_sel:[1,1,0] op_sel_hi:[1,0,1] neg_lo:[0,1,0]\n"
        "v_cvt_pk_bf16_f32 v154, v148, v149\n"
        "v_pk_mul_f32 v[150:151], v[148:149], v[122:123] op_sel:[0,0] op_sel_hi:[0,1]\n"
        "global_load_lds_dwordx4 v15, s[10:11]\n"
        "v_pk_fma_f32 v[148:149], v[148:149], v[122:123], v[150:151] op_sel:[1,1,0] op_sel_hi:[1,0,1] neg_lo:[0,1,0]\n"
        "v_cvt_pk_bf16_f32 v155, v148, v149\n"
        "ds_write2_b32 v100, v154, v155 offset0:32 offset1:36\n"
        "v_pk_mul_f32 v[150:151], v[148:149], v[122:123] op_sel:[0,0] op_sel_hi:[0,1]\n"
        "v_pk_fma_f32 v[148:149], v[148:149], v[122:123], v[150:151] op_sel:[1,1,0] op_sel_hi:[1,0,1] neg_lo:[0,1,0]\n"
        "s_add_u32 m0, s24, 0xc00\n"
        "v_cvt_pk_bf16_f32 v156, v148, v149\n"
        "v_pk_mul_f32 v[150:151], v[148:149], v[122:123] op_sel:[0,0] op_sel_hi:[0,1]\n"
        "v_pk_fma_f32 v[148:149], v[148:149], v[122:123], v[150:151] op_sel:[1,1,0] op_sel_hi:[1,0,1] neg_lo:[0,1,0]\n"
        "v_cvt_pk_bf16_f32 v157, v148, v149\n"
        "ds_write2_b32 v100, v156, v157 offset0:40 offset1:44\n"
        "global_load_lds_dwordx4 v16, s[10:11]\n"
        "v_pk_mul_f32 v[150:151], v[148:149], v[122:123] op_sel:[0,0] op_sel_hi:[0,1]\n"
        "v_pk_fma_f32 v[148:149], v[148:149], v[122:123], v[150:151] op_sel:[1,1,0] op_sel_hi:[1,0,1] neg_lo:[0,1,0]\n"
        "v_mov_b64 v[130:131], 0\n"
        "v_mov_b32 v106, 0x3f803f80\n"
        "s_waitcnt vmcnt(9)\n"
        "v_dot2c_f32_bf16 v130, v48, v106\n"
        "v_dot2c_f32_bf16 v131, v48, v48\n"
        "v_dot2c_f32_bf16 v130, v49, v106\n"
        "v_dot2c_f32_bf16 v131, v49, v49\n"
        "v_dot2c_f32_bf16 v130, v50, v106\n"
        "v_dot2c_f32_bf16 v131, v50, v50\n"
        "v_dot2c_f32_bf16 v130, v51, v106\n"
        "v_dot2c_f32_bf16 v131, v51, v51\n"
        "v_dot2c_f32_bf16 v130, v52, v106\n"
        "v_dot2c_f32_bf16 v131, v52, v52\n"
        "v_dot2c_f32_bf16 v130, v53, v106\n"
        "v_dot2c_f32_bf16 v131, v53, v53\n"
        "v_dot2c_f32_bf16 v130, v54, v106\n"
        "v_dot2c_f32_bf16 v131, v54, v54\n"
        "v_dot2c_f32_bf16 v130, v55, v106\n"
        "v_dot2c_f32_bf16 v131, v55, v55\n"
        "s_waitcnt vmcnt(8)\n"
        "v_readfirstlane_b32 s41, v64\n"
        "s_mov_b32 s49, 0x3b000000\n"
        "s_add_u32 s41, s41, 1\n"
        "v_add_f32_dpp v130, v130, v130 quad_perm:[1,0,3,2] row_mask:0xf bank_mask:0xf bound_ctrl:1\n"
        "v_add_f32_dpp v131, v131, v131 quad_perm:[1,0,3,2] row_mask:0xf bank_mask:0xf bound_ctrl:1\n"
        "s_and_b32 s59, s41, 7\n"
        "v_add_f32_dpp v130, v130, v130 quad_perm:[2,3,0,1] row_mask:0xf bank_mask:0xf bound_ctrl:1\n"
        "v_add_f32_dpp v131, v131, v131 quad_perm:[2,3,0,1] row_mask:0xf bank_mask:0xf bound_ctrl:1\n"
        "s_lshl_b32 s59, s59, 25\n"
        "v_add_f32_dpp v130, v130, v130 row_half_mirror row_mask:0xf bank_mask:0xf bound_ctrl:1\n"
        "v_add_f32_dpp v131, v131, v131 row_half_mirror row_mask:0xf bank_mask:0xf bound_ctrl:1\n"
        "s_add_u32 s36, s36, s59\n"
        "v_add_f32_dpp v130, v130, v130 row_mirror row_mask:0xf bank_mask:0xf bound_ctrl:1\n"
        "v_add_f32_dpp v131, v131, v131 row_mirror row_mask:0xf bank_mask:0xf bound_ctrl:1\n"
        "s_addc_u32 s37, s37, 0\n"
        "v_mov_b32 v106, v130\n"
        "v_mov_b32 v107, v131\n"
        "s_or_b32 s0, s41, s1\n"
        "v_permlane16_swap_b32 v130, v106\n"
        "v_permlane16_swap_b32 v131, v107\n"
        "v_add_f32 v130, v130, v106\n"
        "v_add_f32 v131, v131, v107\n"
        "v_mul_f32 v130, s49, v130\n"
        "v_mul_f32 v106, v130, v130\n"
        "v_fma_f32 v131, v131, s49, -v106\n"
        "v_max_f32 v131, 0, v131\n"
        "v_cvt_pk_bf16_f32 v131, v131, v131\n"
        "v_and_b32 v131, s1, v131\n"
        "v_add_f32 v131, 0x3727c5ac, v131\n"
        "v_cvt_pk_bf16_f32 v131, v131, v131\n"
        "v_and_b32 v131, s1, v131\n"
        "v_sqrt_f32 v131, v131\n"
        "s_nop 0\n"
        "v_cvt_pk_bf16_f32 v130, v130, v131\n"
        "v_mov_b32 v131, s41\n"
        "ds_write_b64 v45, v[130:131]\n"
        "s_lshr_b32 s52, s41, 3\n"
        "v_mad_u32_u24 v222, v3, 3, 3\n"
        "v_lshrrev_b32_e64 v222, v222, s52\n"
        "v_add_u32 v222, s52, v222\n"
        "v_and_b32 v220, 1, v222\n"
        "v_lshlrev_b32 v220, 4, v220\n"
        "v_bfrev_b32 v221, 1\n"
        "v_lshrrev_b32 v220, v220, v221\n"
        "v_lshlrev_b32 v221, 29, v222\n"
        "v_bfe_u32 v222, v222, 1, 1\n"
        "v_and_b32 v221, 0x80000000, v221\n"
        "v_lshl_or_b32 v221, v222, 15, v221\n"
        "v_cvt_pk_bf16_f32 v154, v148, v149\n"
        "v_pk_mul_f32 v[150:151], v[148:149], v[122:123] op_sel:[0,0] op_sel_hi:[0,1]\n"
        "v_pk_fma_f32 v[148:149], v[148:149], v[122:123], v[150:151] op_sel:[1,1,0] op_sel_hi:[1,0,1] neg_lo:[0,1,0]\n"
        "v_cvt_pk_bf16_f32 v155, v148, v149\n"
        "ds_write2_b32 v100, v154, v155 offset0:48 offset1:52\n"
        "v_pk_mul_f32 v[150:151], v[148:149], v[122:123] op_sel:[0,0] op_sel_hi:[0,1]\n"
        "v_pk_fma_f32 v[148:149], v[148:149], v[122:123], v[150:151] op_sel:[1,1,0] op_sel_hi:[1,0,1] neg_lo:[0,1,0]\n"
        "v_cvt_pk_bf16_f32 v156, v148, v149\n"
        "v_pk_mul_f32 v[150:151], v[148:149], v[122:123] op_sel:[0,0] op_sel_hi:[0,1]\n"
        "v_pk_fma_f32 v[148:149], v[148:149], v[122:123], v[150:151] op_sel:[1,1,0] op_sel_hi:[1,0,1] neg_lo:[0,1,0]\n"
        "v_cvt_pk_bf16_f32 v157, v148, v149\n"
        "ds_write2_b32 v100, v156, v157 offset0:56 offset1:60\n"
        "v_pk_mul_f32 v[150:151], v[148:149], v[122:123] op_sel:[0,0] op_sel_hi:[0,1]\n"
        "v_pk_fma_f32 v[148:149], v[148:149], v[122:123], v[150:151] op_sel:[1,1,0] op_sel_hi:[1,0,1] neg_lo:[0,1,0]\n"
        "v_cvt_pk_bf16_f32 v154, v148, v149\n"
        "v_pk_mul_f32 v[150:151], v[148:149], v[122:123] op_sel:[0,0] op_sel_hi:[0,1]\n"
        "v_pk_fma_f32 v[148:149], v[148:149], v[122:123], v[150:151] op_sel:[1,1,0] op_sel_hi:[1,0,1] neg_lo:[0,1,0]\n"
        "v_cvt_pk_bf16_f32 v155, v148, v149\n"
        "ds_write2_b32 v100, v154, v155 offset0:64 offset1:68\n"
        "v_pk_mul_f32 v[150:151], v[148:149], v[122:123] op_sel:[0,0] op_sel_hi:[0,1]\n"
        "v_pk_fma_f32 v[148:149], v[148:149], v[122:123], v[150:151] op_sel:[1,1,0] op_sel_hi:[1,0,1] neg_lo:[0,1,0]\n"
        "v_cvt_pk_bf16_f32 v156, v148, v149\n"
        "v_pk_mul_f32 v[150:151], v[148:149], v[122:123] op_sel:[0,0] op_sel_hi:[0,1]\n"
        "v_pk_fma_f32 v[148:149], v[148:149], v[122:123], v[150:151] op_sel:[1,1,0] op_sel_hi:[1,0,1] neg_lo:[0,1,0]\n"
        "v_cvt_pk_bf16_f32 v157, v148, v149\n"
        "ds_write2_b32 v100, v156, v157 offset0:72 offset1:76\n"
        "v_pk_mul_f32 v[150:151], v[148:149], v[122:123] op_sel:[0,0] op_sel_hi:[0,1]\n"
        "v_pk_fma_f32 v[148:149], v[148:149], v[122:123], v[150:151] op_sel:[1,1,0] op_sel_hi:[1,0,1] neg_lo:[0,1,0]\n"
        "v_cvt_pk_bf16_f32 v154, v148, v149\n"
        "v_pk_mul_f32 v[150:151], v[148:149], v[122:123] op_sel:[0,0] op_sel_hi:[0,1]\n"
        "s_waitcnt lgkmcnt(0)\n"
        "s_barrier\n"
        "v_add_u32 v29, 0x27600, v4\n"
        "ds_read_b128 v[224:227], v29\n"
        "s_cmp_eq_u32 s40, 0\n"
        "s_cselect_b32 s52, 15, 0\n"
        "s_mov_b32 s53, 0\n"
        "s_waitcnt lgkmcnt(0)\n"
        "s_mov_b64 exec, s[52:53]\n"
        "buffer_store_dwordx4 v[224:227], v44, s[36:39], s99 offen sc1\n"
        "s_mov_b64 exec, -1\n"
        "v_pk_fma_f32 v[148:149], v[148:149], v[122:123], v[150:151] op_sel:[1,1,0] op_sel_hi:[1,0,1] neg_lo:[0,1,0]\n"
        "v_cvt_pk_bf16_f32 v155, v148, v149\n"
        "ds_write2_b32 v100, v154, v155 offset0:80 offset1:84\n"
        "v_pk_mul_f32 v[150:151], v[148:149], v[122:123] op_sel:[0,0] op_sel_hi:[0,1]\n"
        "v_pk_fma_f32 v[148:149], v[148:149], v[122:123], v[150:151] op_sel:[1,1,0] op_sel_hi:[1,0,1] neg_lo:[0,1,0]\n"
        "v_cvt_pk_bf16_f32 v156, v148, v149\n"
        "v_pk_mul_f32 v[150:151], v[148:149], v[122:123] op_sel:[0,0] op_sel_hi:[0,1]\n"
        "v_pk_fma_f32 v[148:149], v[148:149], v[122:123], v[150:151] op_sel:[1,1,0] op_sel_hi:[1,0,1] neg_lo:[0,1,0]\n"
        "v_cvt_pk_bf16_f32 v157, v148, v149\n"
        "ds_write2_b32 v100, v156, v157 offset0:88 offset1:92\n"
        "v_mov_b32 v133, s0\n"
        "v_mov_b32 v135, s0\n"
        "v_mov_b32 v137, s0\n"
        "v_mov_b32 v139, s0\n"
        "v_mov_b32 v141, s0\n"
        "v_mov_b32 v143, s0\n"
        "v_mov_b32 v145, s0\n"
        "v_mov_b32 v147, s0\n"
        "s_mov_b32 s49, 0x400000\n"
        "s_waitcnt vmcnt(7)\n"
        "v_mov_b32 v132, v56\n"
        "v_mov_b32 v134, v60\n"
        "buffer_store_dwordx4 v[132:135], v75, s[36:39], s49 offen sc1\n"
        "s_add_u32 s49, s49, s99\n"
        "v_mov_b32 v136, v57\n"
        "v_mov_b32 v138, v61\n"
        "buffer_store_dwordx4 v[136:139], v75, s[36:39], s49 offen sc1\n"
        "s_add_u32 s49, s49, s99\n"
        "v_mov_b32 v140, v58\n"
        "v_mov_b32 v142, v62\n"
        "buffer_store_dwordx4 v[140:143], v75, s[36:39], s49 offen sc1\n"
        "s_add_u32 s49, s49, s99\n"
        "v_mov_b32 v144, v59\n"
        "v_mov_b32 v146, v63\n"
        "buffer_store_dwordx4 v[144:147], v75, s[36:39], s49 offen sc1\n"
        "v_pk_mul_f32 v[150:151], v[148:149], v[122:123] op_sel:[0,0] op_sel_hi:[0,1]\n"
        "v_pk_fma_f32 v[148:149], v[148:149], v[122:123], v[150:151] op_sel:[1,1,0] op_sel_hi:[1,0,1] neg_lo:[0,1,0]\n"
        "v_cvt_pk_bf16_f32 v154, v148, v149\n"
        "v_pk_mul_f32 v[150:151], v[148:149], v[122:123] op_sel:[0,0] op_sel_hi:[0,1]\n"
        "v_pk_fma_f32 v[148:149], v[148:149], v[122:123], v[150:151] op_sel:[1,1,0] op_sel_hi:[1,0,1] neg_lo:[0,1,0]\n"
        "v_cvt_pk_bf16_f32 v155, v148, v149\n"
        "ds_write2_b32 v100, v154, v155 offset0:96 offset1:100\n"
        "v_pk_mul_f32 v[150:151], v[148:149], v[122:123] op_sel:[0,0] op_sel_hi:[0,1]\n"
        "v_pk_fma_f32 v[148:149], v[148:149], v[122:123], v[150:151] op_sel:[1,1,0] op_sel_hi:[1,0,1] neg_lo:[0,1,0]\n"
        "v_cvt_pk_bf16_f32 v156, v148, v149\n"
        "v_pk_mul_f32 v[150:151], v[148:149], v[122:123] op_sel:[0,0] op_sel_hi:[0,1]\n"
        "v_pk_fma_f32 v[148:149], v[148:149], v[122:123], v[150:151] op_sel:[1,1,0] op_sel_hi:[1,0,1] neg_lo:[0,1,0]\n"
        "v_cvt_pk_bf16_f32 v157, v148, v149\n"
        "ds_write2_b32 v100, v156, v157 offset0:104 offset1:108\n"
        "v_pk_mul_f32 v[150:151], v[148:149], v[122:123] op_sel:[0,0] op_sel_hi:[0,1]\n"
        "v_pk_fma_f32 v[148:149], v[148:149], v[122:123], v[150:151] op_sel:[1,1,0] op_sel_hi:[1,0,1] neg_lo:[0,1,0]\n"
        "v_cvt_pk_bf16_f32 v154, v148, v149\n"
        "v_pk_mul_f32 v[150:151], v[148:149], v[122:123] op_sel:[0,0] op_sel_hi:[0,1]\n"
        "s_waitcnt vmcnt(9)\n"
        "ds_read_b64_tr_b16 v[212:213], v40\n"
        "ds_read_b64_tr_b16 v[214:215], v40 offset:1024\n"
        "ds_read_b64_tr_b16 v[216:217], v40 offset:32\n"
        "ds_read_b64_tr_b16 v[218:219], v40 offset:1056\n"
        "v_pk_fma_f32 v[148:149], v[148:149], v[122:123], v[150:151] op_sel:[1,1,0] op_sel_hi:[1,0,1] neg_lo:[0,1,0]\n"
        "v_cvt_pk_bf16_f32 v155, v148, v149\n"
        "ds_write2_b32 v100, v154, v155 offset0:112 offset1:116\n"
        "v_pk_mul_f32 v[150:151], v[148:149], v[122:123] op_sel:[0,0] op_sel_hi:[0,1]\n"
        "v_pk_fma_f32 v[148:149], v[148:149], v[122:123], v[150:151] op_sel:[1,1,0] op_sel_hi:[1,0,1] neg_lo:[0,1,0]\n"
        "v_cvt_pk_bf16_f32 v156, v148, v149\n"
        "v_pk_mul_f32 v[150:151], v[148:149], v[122:123] op_sel:[0,0] op_sel_hi:[0,1]\n"
        "v_pk_fma_f32 v[148:149], v[148:149], v[122:123], v[150:151] op_sel:[1,1,0] op_sel_hi:[1,0,1] neg_lo:[0,1,0]\n"
        "v_cvt_pk_bf16_f32 v157, v148, v149\n"
        "ds_write2_b32 v100, v156, v157 offset0:120 offset1:124\n"
        "v_mul_f32 v68, v46, v68\n"
        "v_mul_f32 v69, v46, v69\n"
        "s_lshl_b32 s51, s63, 11\n"
        "s_add_u32 s51, s51, 0x200000\n"
        "s_waitcnt lgkmcnt(2)\n"
        "v_xor_b32 v212, v220, v212\n"
        "v_xor_b32 v213, v221, v213\n"
        "v_xor_b32 v214, v220, v214\n"
        "v_xor_b32 v215, v221, v215\n"
        "buffer_store_dwordx4 v[212:215], v4, s[36:39], s51 offen sc1\n"
        "v_xor_b32 v216, v220, v216\n"
        "v_xor_b32 v217, v221, v217\n"
        "v_xor_b32 v218, v220, v218\n"
        "v_xor_b32 v219, v221, v219\n"
        "buffer_store_dwordx4 v[216:219], v4, s[36:39], s51 offen offset:1024 sc1\n"
        "v_pk_mul_f32 v[152:153], v[70:71], v[68:69] op_sel:[0,0] op_sel_hi:[0,1]\n"
        "v_pk_fma_f32 v[160:161], v[70:71], v[68:69], v[152:153] op_sel:[1,1,0] op_sel_hi:[1,0,1] neg_lo:[0,1,0]\n"
        "v_cvt_pk_bf16_f32 v202, v160, -v161\n"
        "v_pk_mul_f32 v[152:153], v[160:161], v[120:121] op_sel:[0,0] op_sel_hi:[0,1]\n"
        "v_pk_fma_f32 v[162:163], v[160:161], v[120:121], v[152:153] op_sel:[1,1,0] op_sel_hi:[1,0,1] neg_lo:[0,1,0]\n"
        "v_cvt_pk_bf16_f32 v203, v162, -v163\n"
        "ds_write2_b32 v100, v202, v203 offset0:128 offset1:132\n"
        "v_pk_mul_f32 v[152:153], v[162:163], v[120:121] op_sel:[0,0] op_sel_hi:[0,1]\n"
        "v_pk_fma_f32 v[164:165], v[162:163], v[120:121], v[152:153] op_sel:[1,1,0] op_sel_hi:[1,0,1] neg_lo:[0,1,0]\n"
        "v_cvt_pk_bf16_f32 v204, v164, -v165\n"
        "v_pk_mul_f32 v[152:153], v[164:165], v[120:121] op_sel:[0,0] op_sel_hi:[0,1]\n"
        "v_pk_fma_f32 v[166:167], v[164:165], v[120:121], v[152:153] op_sel:[1,1,0] op_sel_hi:[1,0,1] neg_lo:[0,1,0]\n"
        "v_cvt_pk_bf16_f32 v205, v166, -v167\n"
        "ds_write2_b32 v100, v204, v205 offset0:136 offset1:140\n"
        "v_pk_mul_f32 v[152:153], v[166:167], v[120:121] op_sel:[0,0] op_sel_hi:[0,1]\n"
        "v_pk_fma_f32 v[168:169], v[166:167], v[120:121], v[152:153] op_sel:[1,1,0] op_sel_hi:[1,0,1] neg_lo:[0,1,0]\n"
        "v_cvt_pk_bf16_f32 v202, v168, -v169\n"
        "v_pk_mul_f32 v[152:153], v[168:169], v[120:121] op_sel:[0,0] op_sel_hi:[0,1]\n"
        "v_pk_fma_f32 v[170:171], v[168:169], v[120:121], v[152:153] op_sel:[1,1,0] op_sel_hi:[1,0,1] neg_lo:[0,1,0]\n"
        "v_cvt_pk_bf16_f32 v203, v170, -v171\n"
        "ds_write2_b32 v100, v202, v203 offset0:144 offset1:148\n"
        "v_pk_mul_f32 v[152:153], v[170:171], v[120:121] op_sel:[0,0] op_sel_hi:[0,1]\n"
        "v_pk_fma_f32 v[172:173], v[170:171], v[120:121], v[152:153] op_sel:[1,1,0] op_sel_hi:[1,0,1] neg_lo:[0,1,0]\n"
        "v_cvt_pk_bf16_f32 v204, v172, -v173\n"
        "v_pk_mul_f32 v[152:153], v[172:173], v[120:121] op_sel:[0,0] op_sel_hi:[0,1]\n"
        "v_pk_fma_f32 v[174:175], v[172:173], v[120:121], v[152:153] op_sel:[1,1,0] op_sel_hi:[1,0,1] neg_lo:[0,1,0]\n"
        "v_cvt_pk_bf16_f32 v205, v174, -v175\n"
        "ds_write2_b32 v100, v204, v205 offset0:152 offset1:156\n"
        "v_pk_mul_f32 v[152:153], v[160:161], v[126:127] op_sel:[0,0] op_sel_hi:[0,1]\n"
        "v_pk_fma_f32 v[176:177], v[160:161], v[126:127], v[152:153] op_sel:[1,1,0] op_sel_hi:[1,0,1] neg_lo:[0,1,0]\n"
        "v_cvt_pk_bf16_f32 v202, v176, -v177\n"
        "v_pk_mul_f32 v[152:153], v[164:165], v[126:127] op_sel:[0,0] op_sel_hi:[0,1]\n"
        "v_pk_fma_f32 v[178:179], v[164:165], v[126:127], v[152:153] op_sel:[1,1,0] op_sel_hi:[1,0,1] neg_lo:[0,1,0]\n"
        "v_cvt_pk_bf16_f32 v203, v178, -v179\n"
        "ds_write2_b32 v100, v202, v203 offset0:160 offset1:164\n"
        "v_pk_mul_f32 v[152:153], v[168:169], v[126:127] op_sel:[0,0] op_sel_hi:[0,1]\n"
        "v_pk_fma_f32 v[180:181], v[168:169], v[126:127], v[152:153] op_sel:[1,1,0] op_sel_hi:[1,0,1] neg_lo:[0,1,0]\n"
        "v_cvt_pk_bf16_f32 v204, v180, -v181\n"
        "v_pk_mul_f32 v[152:153], v[172:173], v[126:127] op_sel:[0,0] op_sel_hi:[0,1]\n"
        "v_pk_fma_f32 v[182:183], v[172:173], v[126:127], v[152:153] op_sel:[1,1,0] op_sel_hi:[1,0,1] neg_lo:[0,1,0]\n"
        "v_cvt_pk_bf16_f32 v205, v182, -v183\n"
        "ds_write2_b32 v100, v204, v205 offset0:168 offset1:172\n"
        "v_pk_mul_f32 v[152:153], v[176:177], v[126:127] op_sel:[0,0] op_sel_hi:[0,1]\n"
        "v_pk_fma_f32 v[184:185], v[176:177], v[126:127], v[152:153] op_sel:[1,1,0] op_sel_hi:[1,0,1] neg_lo:[0,1,0]\n"
        "v_cvt_pk_bf16_f32 v202, v184, -v185\n"
        "v_pk_mul_f32 v[152:153], v[180:181], v[126:127] op_sel:[0,0] op_sel_hi:[0,1]\n"
        "s_nop 0\n"
        "s_cbranch_execz .LWARM_RET_%=\n"
        "v_pk_fma_f32 v[186:187], v[180:181], v[126:127], v[152:153] op_sel:[1,1,0] op_sel_hi:[1,0,1] neg_lo:[0,1,0]\n"
        "v_cvt_pk_bf16_f32 v203, v186, -v187\n"
        "ds_write2_b32 v100, v202, v203 offset0:176 offset1:180\n"
        "v_pk_mul_f32 v[152:153], v[184:185], v[126:127] op_sel:[0,0] op_sel_hi:[0,1]\n"
        "v_pk_fma_f32 v[188:189], v[184:185], v[126:127], v[152:153] op_sel:[1,1,0] op_sel_hi:[1,0,1] neg_lo:[0,1,0]\n"
        "v_cvt_pk_bf16_f32 v204, v188, -v189\n"
        "v_pk_mul_f32 v[152:153], v[186:187], v[126:127] op_sel:[0,0] op_sel_hi:[0,1]\n"
        "v_pk_fma_f32 v[190:191], v[186:187], v[126:127], v[152:153] op_sel:[1,1,0] op_sel_hi:[1,0,1] neg_lo:[0,1,0]\n"
        "v_cvt_pk_bf16_f32 v205, v190, -v191\n"
        "ds_write2_b32 v100, v204, v205 offset0:184 offset1:188\n"
        "v_pk_mul_f32 v[152:153], v[188:189], v[126:127] op_sel:[0,0] op_sel_hi:[0,1]\n"
        "v_pk_fma_f32 v[192:193], v[188:189], v[126:127], v[152:153] op_sel:[1,1,0] op_sel_hi:[1,0,1] neg_lo:[0,1,0]\n"
        "v_cvt_pk_bf16_f32 v202, v192, -v193\n"
        "v_pk_mul_f32 v[152:153], v[192:193], v[126:127] op_sel:[0,0] op_sel_hi:[0,1]\n"
        "v_pk_fma_f32 v[194:195], v[192:193], v[126:127], v[152:153] op_sel:[1,1,0] op_sel_hi:[1,0,1] neg_lo:[0,1,0]\n"
        "v_cvt_pk_bf16_f32 v203, v194, -v195\n"
        "ds_write2_b32 v100, v202, v203 offset0:192 offset1:196\n"
        "v_pk_mul_f32 v[152:153], v[194:195], v[126:127] op_sel:[0,0] op_sel_hi:[0,1]\n"
        "v_pk_fma_f32 v[196:197], v[194:195], v[126:127], v[152:153] op_sel:[1,1,0] op_sel_hi:[1,0,1] neg_lo:[0,1,0]\n"
        "v_cvt_pk_bf16_f32 v204, v196, -v197\n"
        "v_pk_mul_f32 v[152:153], v[196:197], v[126:127] op_sel:[0,0] op_sel_hi:[0,1]\n"
        "v_pk_fma_f32 v[198:199], v[196:197], v[126:127], v[152:153] op_sel:[1,1,0] op_sel_hi:[1,0,1] neg_lo:[0,1,0]\n"
        "v_cvt_pk_bf16_f32 v205, v198, -v199\n"
        "ds_write2_b32 v100, v204, v205 offset0:200 offset1:204\n"
        "v_pk_mul_f32 v[152:153], v[198:199], v[126:127] op_sel:[0,0] op_sel_hi:[0,1]\n"
        "v_pk_fma_f32 v[200:201], v[198:199], v[126:127], v[152:153] op_sel:[1,1,0] op_sel_hi:[1,0,1] neg_lo:[0,1,0]\n"
        "v_cvt_pk_bf16_f32 v202, v200, -v201\n"
        "ds_write_b32 v100, v202 offset:832\n"
        "s_waitcnt lgkmcnt(0)\n"
        "s_barrier\n"
        "ds_read_b128 v[106:109], v105\n"
        "s_lshl_b32 s50, s40, 10\n"
        "ds_read_b128 v[110:113], v101 offset:512\n"
        "s_add_u32 s50, s50, 0x242e0\n"
        "ds_read_b128 v[114:117], v101 offset:768\n"
        "s_add_u32 m0, s24, 0x4000\n"
        "ds_read_b128 v[118:121], v105 offset:848\n"
        "v_lshl_add_u32 v9, v1, 2, s50\n"
        "ds_read_b128 v[122:125], v101 offset:1360\n"
        "ds_read_b128 v[126:129], v101 offset:1616\n"
        "s_and_b32 s50, s40, 1\n"
        "ds_read_b128 v[130:133], v105 offset:1696\n"
        "s_lshl_b32 s50, s50, 8\n"
        "ds_read_b128 v[134:137], v101 offset:2208\n"
        "ds_read_b128 v[138:141], v101 offset:2464\n"
        "s_mul_i32 s51, s42, 0x1200\n"
        "global_load_lds_dwordx4 v13, s[12:13]\n"
        "s_waitcnt lgkmcnt(6)\n"
        "s_sub_u32 s51, s51, s50\n"
        "v_mfma_f32_16x16x32_bf16 v[204:207], v[110:113], v[106:109], 0\n"
        "v_mfma_f32_16x16x32_bf16 v[208:211], v[114:117], v[106:109], 0\n"
        "s_add_u32 s51, s51, 0x24200\n"
        "ds_read_b128 v[142:145], v105 offset:2544\n"
        "ds_read_b128 v[146:149], v101 offset:3056\n"
        "v_lshl_add_u32 v24, v2, 5, s51\n"
        "ds_read_b128 v[150:153], v101 offset:3312\n"
        "s_add_u32 m0, s24, 0x8000\n"
        "v_lshl_add_u32 v12, v3, 4, v24\n"
        "s_waitcnt lgkmcnt(6)\n"
        "v_mfma_f32_16x16x32_bf16 v[204:207], v[122:125], v[118:121], v[204:207]\n"
        "s_lshl2_add_u32 s49, s46, s40\n"
        "v_mfma_f32_16x16x32_bf16 v[208:211], v[126:129], v[118:121], v[208:211]\n"
        "s_mul_i32 s19, s49, 0xe000\n"
        "ds_read_b128 v[154:157], v105 offset:3392\n"
        "ds_read_b128 v[158:161], v101 offset:3904\n"
        "s_add_u32 s19, s19, 0x200000\n"
        "ds_read_b128 v[162:165], v101 offset:4160\n"
        "global_load_lds_dwordx4 v13, s[14:15]\n"
        "s_waitcnt lgkmcnt(6)\n"
        "s_add_u32 s51, s46, s5\n"
        "v_mfma_f32_16x16x32_bf16 v[204:207], v[134:137], v[130:133], v[204:207]\n"
        "s_add_u32 s50, s51, 0\n"
        "v_mfma_f32_16x16x32_bf16 v[208:211], v[138:141], v[130:133], v[208:211]\n"
        "ds_read_b128 v[166:169], v105 offset:4240\n"
        "s_and_b32 s50, s50, 3\n"
        "ds_read_b128 v[170:173], v101 offset:4752\n"
        "s_lshl_b32 s50, s50, 12\n"
        "ds_read_b128 v[174:177], v101 offset:5008\n"
        "s_add_u32 m0, s24, 0xc000\n"
        "s_waitcnt lgkmcnt(6)\n"
        "s_add_u32 s92, s19, s50\n"
        "v_mfma_f32_16x16x32_bf16 v[204:207], v[146:149], v[142:145], v[204:207]\n"
        "v_mfma_f32_16x16x32_bf16 v[208:211], v[150:153], v[142:145], v[208:211]\n"
        "s_add_u32 s50, s51, 1\n"
        "ds_read_b128 v[178:181], v105 offset:5088\n"
        "s_and_b32 s50, s50, 3\n"
        "ds_read_b128 v[182:185], v101 offset:5600\n"
        "ds_read_b128 v[186:189], v101 offset:5856\n"
        "s_lshl_b32 s50, s50, 12\n"
        "global_load_lds_dwordx4 v13, s[16:17]\n"
        "s_waitcnt lgkmcnt(6)\n"
        "s_add_u32 s93, s19, s50\n"
        "v_mfma_f32_16x16x32_bf16 v[204:207], v[158:161], v[154:157], v[204:207]\n"
        "v_mfma_f32_16x16x32_bf16 v[208:211], v[162:165], v[154:157], v[208:211]\n"
        "s_add_u32 s50, s51, 2\n"
        "ds_read_b128 v[190:193], v105 offset:5936\n"
        "ds_read_b128 v[194:197], v101 offset:6448\n"
        "s_and_b32 s50, s50, 3\n"
        "ds_read_b128 v[198:201], v101 offset:6704\n"
        "s_add_u32 m0, s24, 0x4400\n"
        "s_lshl_b32 s50, s50, 12\n"
        "s_waitcnt lgkmcnt(6)\n"
        "v_mfma_f32_16x16x32_bf16 v[204:207], v[170:173], v[166:169], v[204:207]\n"
        "s_add_u32 s94, s19, s50\n"
        "v_mfma_f32_16x16x32_bf16 v[208:211], v[174:177], v[166:169], v[208:211]\n"
        "s_add_u32 s50, s51, 3\n"
        "s_waitcnt lgkmcnt(3)\n"
        "v_mfma_f32_16x16x32_bf16 v[204:207], v[182:185], v[178:181], v[204:207]\n"
        "s_and_b32 s50, s50, 3\n"
        "v_mfma_f32_16x16x32_bf16 v[208:211], v[186:189], v[178:181], v[208:211]\n"
        "global_load_lds_dwordx4 v14, s[12:13]\n"
        "s_waitcnt lgkmcnt(0)\n"
        "s_lshl_b32 s50, s50, 12\n"
        "v_mfma_f32_16x16x32_bf16 v[204:207], v[194:197], v[190:193], v[204:207]\n"
        "s_add_u32 s95, s19, s50\n"
        "v_mfma_f32_16x16x32_bf16 v[208:211], v[198:201], v[190:193], v[208:211]\n"
        "s_nop 5\n"
        "v_cvt_pk_bf16_f32 v24, v204, v205\n"
        "v_cvt_pk_bf16_f32 v25, v206, v207\n"
        "ds_write_b32 v102, v24\n"
        "ds_write_b32 v103, v25\n"
        "v_cvt_pk_bf16_f32 v26, v208, v209\n"
        "v_cvt_pk_bf16_f32 v27, v210, v211\n"
        "ds_write_b16 v104, v26 offset:1024\n"
        "ds_write_b16_d16_hi v104, v26 offset:1088\n"
        "ds_write_b16 v104, v27 offset:1152\n"
        "ds_write_b16_d16_hi v104, v27 offset:1216\n"
        "s_waitcnt lgkmcnt(0)\n"
        "s_barrier\n"
        "ds_read_u16 v106, v82\n"
        "buffer_load_dwordx4 v[224:227], v8, s[36:39], s99 offen sc1\n"
        "ds_read_u16 v107, v83\n"
        "ds_read_u16 v108, v84\n"
        "buffer_load_dwordx4 v[228:231], v8, s[36:39], s99 offen offset:1024 sc1\n"
        "ds_read_u16 v109, v85\n"
        "buffer_load_dwordx4 v[232:235], v8, s[36:39], s99 offen offset:2048 sc1\n"
        "ds_read_u16 v110, v86\n"
        "ds_read_u16 v111, v87\n"
        "buffer_load_dwordx4 v[236:239], v8, s[36:39], s99 offen offset:3072 sc1\n"
        "ds_read_u16 v112, v88\n"
        "ds_read_u16 v113, v89\n"
        "buffer_load_dwordx4 v[240:243], v8, s[36:39], s98 offen sc1\n"
        "ds_read_u16 v114, v90\n"
        "s_add_u32 m0, s24, 0x8400\n"
        "ds_read_u16 v115, v91\n"
        "ds_read_u16 v116, v92\n"
        "global_load_lds_dwordx4 v14, s[14:15]\n"
        "ds_read_u16 v117, v93\n"
        "ds_read_u16 v118, v94\n"
        "s_add_u32 m0, s24, 0xc400\n"
        "ds_read_u16 v119, v95\n"
        "global_load_lds_dwordx4 v14, s[16:17]\n"
        "ds_read_u16 v120, v96\n"
        "ds_read_u16 v121, v97\n"
        "buffer_load_dwordx4 v[244:247], v8, s[36:39], s98 offen offset:1024 sc1\n"
        "ds_read_u16 v122, v98\n"
        "ds_read_u16 v123, v99\n"
        "s_add_u32 m0, s24, 0x4800\n"
        "s_waitcnt lgkmcnt(9)\n"
        "global_load_lds_dwordx4 v15, s[12:13]\n"
        "v_lshl_or_b32 v124, v108, 16, v107\n"
        "v_lshl_or_b32 v125, v110, 16, v109\n"
        "s_add_u32 m0, s24, 0x8800\n"
        "v_lshl_or_b32 v126, v112, 16, v111\n"
        "v_lshl_or_b32 v127, v114, 16, v113\n"
        "global_load_lds_dwordx4 v15, s[14:15]\n"
        "ds_write_b128 v43, v[124:127]\n"
        "buffer_load_dwordx4 v[248:251], v8, s[36:39], s98 offen offset:2048 sc1\n"
        "v_lshl_or_b32 v128, v107, 16, v106\n"
        "v_lshl_or_b32 v129, v109, 16, v108\n"
        "s_add_u32 m0, s24, 0xc800\n"
        "v_lshl_or_b32 v130, v111, 16, v110\n"
        "v_lshl_or_b32 v131, v113, 16, v112\n"
        "global_load_lds_dwordx4 v15, s[16:17]\n"
        "ds_write_b128 v43, v[128:131] offset:4160\n"
        "s_add_u32 m0, s24, 0x4c00\n"
        "s_waitcnt lgkmcnt(2)\n"
        "v_lshl_or_b32 v132, v117, 16, v116\n"
        "global_load_lds_dwordx4 v16, s[12:13]\n"
        "v_lshl_or_b32 v133, v119, 16, v118\n"
        "v_lshl_or_b32 v134, v121, 16, v120\n"
        "buffer_load_dwordx4 v[252:255], v8, s[36:39], s98 offen offset:3072 sc1\n"
        "v_lshl_or_b32 v135, v123, 16, v122\n"
        "s_add_u32 m0, s24, 0x8c00\n"
        "ds_write_b128 v43, v[132:135] offset:2048\n"
        "v_lshl_or_b32 v136, v116, 16, v115\n"
        "global_load_lds_dwordx4 v16, s[14:15]\n"
        "v_lshl_or_b32 v137, v118, 16, v117\n"
        "v_lshl_or_b32 v138, v120, 16, v119\n"
        "s_add_u32 m0, s24, 0xcc00\n"
        "v_lshl_or_b32 v139, v122, 16, v121\n"
        "global_load_lds_dwordx4 v16, s[16:17]\n"
        "ds_write_b128 v43, v[136:139] offset:6208\n"
        "s_waitcnt vmcnt(20)\n"
        "s_waitcnt lgkmcnt(0)\n"
        "s_barrier\n"
        "s_lshl_b32 s49, s2, 6\n"
        "s_add_u32 s49, s49, 0x8000\n"
        "v_mov_b32 v29, s41\n"
        "v_lshlrev_b32 v28, 2, v1\n"
        "s_cmp_eq_u32 s40, 0\n"
        "s_cselect_b32 s52, 0xffff, 0\n"
        "s_mov_b32 s53, 0\n"
        "s_mov_b64 exec, s[52:53]\n"
        "buffer_store_dword v29, v28, s[36:39], s49 offen sc1\n"
        "s_mov_b64 exec, -1\n"
        "ds_read2_b32 a[160:161], v36 offset0:247 offset1:248\n"
        "s_add_u32 m0, s24, 0x10000\n"
        "ds_read2_b32 a[162:163], v36 offset0:249 offset1:250\n"
        "v_add_u32 v102, 0x6000, v4\n"
        "ds_read2_b32 a[164:165], v36 offset0:119 offset1:120\n"
        "global_load_lds_dwordx4 v17, s[10:11]\n"
        "ds_read2_b32 a[166:167], v36 offset0:121 offset1:122\n"
        "ds_read2_b32 a[168:169], v37 offset0:247 offset1:248\n"
        "s_add_u32 m0, s24, 0x10400\n"
        "ds_read2_b32 a[170:171], v37 offset0:249 offset1:250\n"
        "v_add_u32 v103, 0xa000, v4\n"
        "ds_read2_b32 a[172:173], v37 offset0:119 offset1:120\n"
        "global_load_lds_dwordx4 v18, s[10:11]\n"
        "ds_read2_b32 a[174:175], v37 offset0:121 offset1:122\n"
        "ds_read2_b32 a[176:177], v38 offset0:247 offset1:248\n"
        "s_add_u32 m0, s24, 0x10800\n"
        "ds_read2_b32 a[178:179], v38 offset0:249 offset1:250\n"
        "s_add_u32 s26, s19, 0x4000\n"
        "ds_read2_b32 a[180:181], v38 offset0:119 offset1:120\n"
        "global_load_lds_dwordx4 v19, s[10:11]\n"
        "ds_read2_b32 a[182:183], v38 offset0:121 offset1:122\n"
        "ds_read2_b32 a[184:185], v39 offset0:247 offset1:248\n"
        "s_add_u32 m0, s24, 0x10c00\n"
        "ds_read2_b32 a[186:187], v39 offset0:249 offset1:250\n"
        "s_add_u32 s27, s19, 0x5000\n"
        "ds_read2_b32 a[188:189], v39 offset0:119 offset1:120\n"
        "global_load_lds_dwordx4 v20, s[10:11]\n"
        "ds_read2_b32 a[190:191], v39 offset0:121 offset1:122\n"
        "s_getpc_b64 s[52:53]\n"
        "s_and_b32 s52, s52, 0xfffff000\n"
        "s_add_u32 s49, s40, 1\n"
        "s_lshl_b32 s49, s49, 12\n"
        "s_add_u32 s52, s52, s49\n"
        "s_addc_u32 s53, s53, 0\n"
        "s_mov_b64 exec, 0\n"
        "s_setpc_b64 s[52:53]\n"
        ".LWARM_RET_%=:\n"
        "s_add_u32 s49, s49, 0x4000\n"
        "s_cmp_gt_u32 s49, 0x4000\n"
        "s_cbranch_scc1 .LWARM_END_%=\n"
        "s_add_u32 s52, s52, 0x4000\n"
        "s_addc_u32 s53, s53, 0\n"
        "s_setpc_b64 s[52:53]\n"
        ".LWARM_END_%=:\n"
        "s_mov_b64 exec, -1\n"
        "s_waitcnt vmcnt(17)\n"
        "v_bitop3_b32 v24, v225, v227, s41 bitop3:0x7e\n"
        "v_bitop3_b32 v25, v229, v231, s41 bitop3:0x7e\n"
        "v_bitop3_b32 v26, v233, v235, s41 bitop3:0x7e\n"
        "v_bitop3_b32 v27, v237, v239, s41 bitop3:0x7e\n"
        "v_or3_b32 v24, v24, v25, v26\n"
        "v_or_b32 v24, v24, v27\n"
        "v_cmp_ne_u32 vcc, 0, v24\n"
        "s_nop 0\n"
        "s_cbranch_vccnz .LX1s_RETRY_%=\n"
        ".LX1s_DONE_%=:\n"
        "v_and_b32 v107, s1, v224\n"
        "v_rcp_f32 v107, v107\n"
        "v_lshlrev_b32 v106, 16, v224\n"
        "v_and_b32 v109, s1, v226\n"
        "v_rcp_f32 v109, v109\n"
        "v_lshlrev_b32 v108, 16, v226\n"
        "v_and_b32 v111, s1, v228\n"
        "v_rcp_f32 v111, v111\n"
        "v_lshlrev_b32 v110, 16, v228\n"
        "v_and_b32 v113, s1, v230\n"
        "v_rcp_f32 v113, v113\n"
        "v_lshlrev_b32 v112, 16, v230\n"
        "v_and_b32 v115, s1, v232\n"
        "v_rcp_f32 v115, v115\n"
        "v_lshlrev_b32 v114, 16, v232\n"
        "v_and_b32 v117, s1, v234\n"
        "v_rcp_f32 v117, v117\n"
        "v_lshlrev_b32 v116, 16, v234\n"
        "v_and_b32 v119, s1, v236\n"
        "v_rcp_f32 v119, v119\n"
        "v_lshlrev_b32 v118, 16, v236\n"
        "v_and_b32 v121, s1, v238\n"
        "v_rcp_f32 v121, v121\n"
        "v_lshlrev_b32 v120, 16, v238\n"
        "s_waitcnt vmcnt(7)\n"
        "v_bitop3_b32 v28, v241, v243, s0 bitop3:0x7e\n"
        "v_bitop3_b32 v29, v245, v247, s0 bitop3:0x7e\n"
        "v_bitop3_b32 v30, v249, v251, s0 bitop3:0x7e\n"
        "v_bitop3_b32 v31, v253, v255, s0 bitop3:0x7e\n"
        "v_or3_b32 v28, v28, v29, v30\n"
        "v_or_b32 v28, v28, v31\n"
        "v_cmp_ne_u32 vcc, 0, v28\n"
        "s_nop 0\n"
        "s_cbranch_vccnz .LX1x_RETRY_%=\n"
        ".LX1x_DONE_%=:\n"
        "v_lshlrev_b32 v150, 16, v240\n"
        "v_and_b32 v151, s1, v240\n"
        "v_pk_add_f32 v[166:167], v[150:151], v[106:107] op_sel:[0,0] op_sel_hi:[1,0] neg_lo:[0,1] neg_hi:[0,1]\n"
        "v_pk_mul_f32 v[166:167], v[166:167], v[106:107] op_sel:[0,1] op_sel_hi:[1,1]\n"
        "v_lshlrev_b32 v152, 16, v242\n"
        "v_and_b32 v153, s1, v242\n"
        "v_pk_add_f32 v[168:169], v[152:153], v[108:109] op_sel:[0,0] op_sel_hi:[1,0] neg_lo:[0,1] neg_hi:[0,1]\n"
        "v_pk_mul_f32 v[168:169], v[168:169], v[108:109] op_sel:[0,1] op_sel_hi:[1,1]\n"
        "v_lshlrev_b32 v154, 16, v244\n"
        "v_and_b32 v155, s1, v244\n"
        "v_pk_add_f32 v[170:171], v[154:155], v[110:111] op_sel:[0,0] op_sel_hi:[1,0] neg_lo:[0,1] neg_hi:[0,1]\n"
        "v_pk_mul_f32 v[170:171], v[170:171], v[110:111] op_sel:[0,1] op_sel_hi:[1,1]\n"
        "v_lshlrev_b32 v156, 16, v246\n"
        "v_and_b32 v157, s1, v246\n"
        "v_pk_add_f32 v[172:173], v[156:157], v[112:113] op_sel:[0,0] op_sel_hi:[1,0] neg_lo:[0,1] neg_hi:[0,1]\n"
        "v_pk_mul_f32 v[172:173], v[172:173], v[112:113] op_sel:[0,1] op_sel_hi:[1,1]\n"
        "v_cvt_pk_bf16_f32 v122, v166, v168\n"
        "v_cvt_pk_bf16_f32 v123, v170, v172\n"
        "v_cvt_pk_bf16_f32 v124, v167, v169\n"
        "v_cvt_pk_bf16_f32 v125, v171, v173\n"
        "ds_write2st64_b32 v9, v122, v123 offset0:0 offset1:1\n"
        "ds_write2st64_b32 v9, v124, v125 offset0:18 offset1:19\n"
        "v_lshlrev_b32 v158, 16, v248\n"
        "v_and_b32 v159, s1, v248\n"
        "v_pk_add_f32 v[174:175], v[158:159], v[114:115] op_sel:[0,0] op_sel_hi:[1,0] neg_lo:[0,1] neg_hi:[0,1]\n"
        "v_pk_mul_f32 v[174:175], v[174:175], v[114:115] op_sel:[0,1] op_sel_hi:[1,1]\n"
        "v_lshlrev_b32 v160, 16, v250\n"
        "v_and_b32 v161, s1, v250\n"
        "v_pk_add_f32 v[176:177], v[160:161], v[116:117] op_sel:[0,0] op_sel_hi:[1,0] neg_lo:[0,1] neg_hi:[0,1]\n"
        "v_pk_mul_f32 v[176:177], v[176:177], v[116:117] op_sel:[0,1] op_sel_hi:[1,1]\n"
        "v_lshlrev_b32 v162, 16, v252\n"
        "v_and_b32 v163, s1, v252\n"
        "v_pk_add_f32 v[178:179], v[162:163], v[118:119] op_sel:[0,0] op_sel_hi:[1,0] neg_lo:[0,1] neg_hi:[0,1]\n"
        "v_pk_mul_f32 v[178:179], v[178:179], v[118:119] op_sel:[0,1] op_sel_hi:[1,1]\n"
        "v_lshlrev_b32 v164, 16, v254\n"
        "v_and_b32 v165, s1, v254\n"
        "v_pk_add_f32 v[180:181], v[164:165], v[120:121] op_sel:[0,0] op_sel_hi:[1,0] neg_lo:[0,1] neg_hi:[0,1]\n"
        "v_pk_mul_f32 v[180:181], v[180:181], v[120:121] op_sel:[0,1] op_sel_hi:[1,1]\n"
        "v_cvt_pk_bf16_f32 v126, v174, v176\n"
        "v_cvt_pk_bf16_f32 v127, v178, v180\n"
        "v_cvt_pk_bf16_f32 v128, v175, v177\n"
        "v_cvt_pk_bf16_f32 v129, v179, v181\n"
        "ds_write2st64_b32 v9, v126, v127 offset0:2 offset1:3\n"
        "ds_write2st64_b32 v9, v128, v129 offset0:20 offset1:21\n"
        "s_waitcnt lgkmcnt(0)\n"
        "s_barrier\n"
        "ds_read_b128 v[192:195], v12 offset:192\n"
        "ds_read_b128 v[196:199], v12 offset:704\n"
        "ds_read_b128 v[200:203], v12 offset:1216\n"
        "ds_read_b128 v[204:207], v12 offset:1728\n"
        "ds_read_b128 v[208:211], v12 offset:2240\n"
        "ds_read_b128 v[212:215], v12 offset:2752\n"
        "ds_read_b128 v[216:219], v12 offset:3264\n"
        "ds_read_b128 v[220:223], v12 offset:3776\n"
        "s_waitcnt lgkmcnt(7)\n"
        "v_mfma_f32_16x16x32_bf16 a[224:227], a[160:163], v[192:195], 0\n"
        "s_waitcnt lgkmcnt(6)\n"
        "v_mfma_f32_16x16x32_bf16 a[228:231], a[160:163], v[196:199], 0\n"
        "s_mul_i32 s59, s44, 0xe0\n"
        "s_waitcnt lgkmcnt(5)\n"
        "v_mfma_f32_16x16x32_bf16 a[232:235], a[160:163], v[200:203], 0\n"
        "v_lshl_add_u32 v31, v1, 1, s59\n"
        "s_waitcnt lgkmcnt(4)\n"
        "v_mfma_f32_16x16x32_bf16 a[236:239], a[160:163], v[204:207], 0\n"
        "s_waitcnt lgkmcnt(3)\n"
        "v_mfma_f32_16x16x32_bf16 a[240:243], a[160:163], v[208:211], 0\n"
        "global_load_ushort v21, v31, s[28:29]\n"
        "ds_read_b128 v[224:227], v12 offset:128\n"
        "s_waitcnt lgkmcnt(3)\n"
        "v_mfma_f32_16x16x32_bf16 a[244:247], a[160:163], v[212:215], 0\n"
        "ds_read_b128 v[228:231], v12 offset:640\n"
        "s_waitcnt lgkmcnt(3)\n"
        "v_mfma_f32_16x16x32_bf16 a[248:251], a[160:163], v[216:219], 0\n"
        "ds_read_b128 v[232:235], v12 offset:1152\n"
        "v_min_u32 v31, 2, v3\n"
        "s_waitcnt lgkmcnt(3)\n"
        "v_mfma_f32_16x16x32_bf16 a[252:255], a[160:163], v[220:223], 0\n"
        "ds_read_b128 v[236:239], v12 offset:1664\n"
        "v_mfma_f32_16x16x32_bf16 a[228:231], a[164:167], v[192:195], a[228:231]\n"
        "ds_read_b128 v[240:243], v12 offset:2176\n"
        "v_lshl_add_u32 v31, v31, 4, v2\n"
        "v_fma_f32 v48, v166, s60, v150\n"
        "v_mfma_f32_16x16x32_bf16 a[232:235], a[164:167], v[196:199], a[232:235]\n"
        "ds_read_b128 v[244:247], v12 offset:2688\n"
        "v_fma_f32 v56, v167, s61, v151\n"
        "v_mfma_f32_16x16x32_bf16 a[236:239], a[164:167], v[200:203], a[236:239]\n"
        "ds_read_b128 v[248:251], v12 offset:3200\n"
        "v_lshl_add_u32 v31, v31, 1, s59\n"
        "v_fma_f32 v49, v168, s60, v152\n"
        "v_mfma_f32_16x16x32_bf16 a[240:243], a[164:167], v[204:207], a[240:243]\n"
        "ds_read_b128 v[252:255], v12 offset:3712\n"
        "v_fma_f32 v57, v169, s61, v153\n"
        "v_mfma_f32_16x16x32_bf16 a[244:247], a[164:167], v[208:211], a[244:247]\n"
        "global_load_ushort v22, v31, s[28:29] offset:128\n"
        "ds_read2_b32 a[192:193], v36 offset0:231 offset1:232\n"
        "v_fma_f32 v50, v170, s60, v154\n"
        "v_mfma_f32_16x16x32_bf16 a[248:251], a[164:167], v[212:215], a[248:251]\n"
        "ds_read2_b32 a[194:195], v36 offset0:233 offset1:234\n"
        "v_fma_f32 v58, v171, s61, v155\n"
        "v_mfma_f32_16x16x32_bf16 a[252:255], a[164:167], v[216:219], a[252:255]\n"
        "s_add_u32 m0, s24, 0x14000\n"
        "ds_read2_b32 a[196:197], v36 offset0:103 offset1:104\n"
        "v_mfma_f32_16x16x32_bf16 a[232:235], a[168:171], v[192:195], a[232:235]\n"
        "ds_read2_b32 a[198:199], v36 offset0:105 offset1:106\n"
        "v_fma_f32 v51, v172, s60, v156\n"
        "v_mfma_f32_16x16x32_bf16 a[236:239], a[168:171], v[196:199], a[236:239]\n"
        "global_load_lds_dwordx4 v17, s[12:13]\n"
        "v_fma_f32 v59, v173, s61, v157\n"
        "v_mfma_f32_16x16x32_bf16 a[240:243], a[168:171], v[200:203], a[240:243]\n"
        "ds_read2_b32 a[200:201], v37 offset0:231 offset1:232\n"
        "v_fma_f32 v52, v174, s60, v158\n"
        "v_mfma_f32_16x16x32_bf16 a[244:247], a[168:171], v[204:207], a[244:247]\n"
        "s_add_u32 m0, s24, 0x18000\n"
        "ds_read2_b32 a[202:203], v37 offset0:233 offset1:234\n"
        "v_fma_f32 v60, v175, s61, v159\n"
        "v_mfma_f32_16x16x32_bf16 a[248:251], a[168:171], v[208:211], a[248:251]\n"
        "ds_read2_b32 a[204:205], v37 offset0:103 offset1:104\n"
        "v_fma_f32 v53, v176, s60, v160\n"
        "v_mfma_f32_16x16x32_bf16 a[252:255], a[168:171], v[212:215], a[252:255]\n"
        "global_load_lds_dwordx4 v17, s[14:15]\n"
        "s_lshl2_add_u32 s52, s43, s40\n"
        "ds_read2_b32 a[206:207], v37 offset0:105 offset1:106\n"
        "v_fma_f32 v61, v177, s61, v161\n"
        "v_mfma_f32_16x16x32_bf16 a[236:239], a[172:175], v[192:195], a[236:239]\n"
        "s_lshl_b32 s25, s52, 7\n"
        "ds_read2_b32 a[208:209], v38 offset0:231 offset1:232\n"
        "v_mfma_f32_16x16x32_bf16 a[240:243], a[172:175], v[196:199], a[240:243]\n"
        "s_add_u32 m0, s24, 0x1c000\n"
        "ds_read2_b32 a[210:211], v38 offset0:233 offset1:234\n"
        "v_fma_f32 v54, v178, s60, v162\n"
        "v_mfma_f32_16x16x32_bf16 a[244:247], a[172:175], v[200:203], a[244:247]\n"
        "s_add_u32 s25, s25, 0x400000\n"
        "ds_read2_b32 a[212:213], v38 offset0:103 offset1:104\n"
        "v_fma_f32 v62, v179, s61, v163\n"
        "v_mfma_f32_16x16x32_bf16 a[248:251], a[172:175], v[204:207], a[248:251]\n"
        "global_load_lds_dwordx4 v17, s[16:17]\n"
        "ds_read2_b32 a[214:215], v38 offset0:105 offset1:106\n"
        "v_fma_f32 v55, v180, s60, v164\n"
        "v_mfma_f32_16x16x32_bf16 a[252:255], a[172:175], v[208:211], a[252:255]\n"
        "s_lshl_b32 s53, s7, 19\n"
        "ds_read2_b32 a[216:217], v39 offset0:231 offset1:232\n"
        "v_fma_f32 v63, v181, s61, v165\n"
        "v_mfma_f32_16x16x32_bf16 a[240:243], a[176:179], v[192:195], a[240:243]\n"
        "s_add_u32 m0, s24, 0x14400\n"
        "s_mul_i32 s49, s40, 0x800\n"
        "ds_read2_b32 a[218:219], v39 offset0:233 offset1:234\n"
        "v_mfma_f32_16x16x32_bf16 a[244:247], a[176:179], v[196:199], a[244:247]\n"
        "s_add_u32 s8, s25, s53\n"
        "ds_read2_b32 a[220:221], v39 offset0:103 offset1:104\n"
        "v_lshl_add_u32 v25, v3, 2, v2\n"
        "v_mfma_f32_16x16x32_bf16 a[248:251], a[176:179], v[200:203], a[248:251]\n"
        "global_load_lds_dwordx4 v18, s[12:13]\n"
        "s_add_u32 s53, s53, 0x80000\n"
        "ds_read2_b32 a[222:223], v39 offset0:105 offset1:106\n"
        "v_mfma_f32_16x16x32_bf16 a[252:255], a[176:179], v[204:207], a[252:255]\n"
        "v_and_b32 v25, 15, v25\n"
        "v_mfma_f32_16x16x32_bf16 a[244:247], a[180:183], v[192:195], a[244:247]\n"
        "s_lshl_b32 s51, s40, 12\n"
        "s_and_b32 s53, s53, 0x3fffff\n"
        "v_lshl_or_b32 v25, v3, 4, v25\n"
        "v_mfma_f32_16x16x32_bf16 a[248:251], a[180:183], v[196:199], a[248:251]\n"
        "v_lshl_add_u32 v10, v25, 4, s49\n"
        "v_mfma_f32_16x16x32_bf16 a[252:255], a[180:183], v[200:203], a[252:255]\n"
        "s_add_u32 s51, s51, 0x8000\n"
        "s_add_u32 s9, s25, s53\n"
        "v_add_u32 v10, 0x20000, v10\n"
        "v_mfma_f32_16x16x32_bf16 a[248:251], a[184:187], v[192:195], a[248:251]\n"
        "v_lshl_add_u32 v7, v1, 3, s49\n"
        "v_mfma_f32_16x16x32_bf16 a[252:255], a[184:187], v[196:199], a[252:255]\n"
        "v_lshlrev_b32 v30, 6, v1\n"
        "s_add_u32 s53, s53, 0x80000\n"
        "s_mul_i32 s49, s40, 0x2000\n"
        "v_mfma_f32_16x16x32_bf16 a[252:255], a[188:191], v[192:195], a[252:255]\n"
        "buffer_load_dword v99, v30, s[36:39], s51 offen sc1\n"
        "s_waitcnt lgkmcnt(0)\n"
        "v_mfma_f32_16x16x32_bf16 a[224:227], a[192:195], v[224:227], a[224:227]\n"
        "s_and_b32 s53, s53, 0x3fffff\n"
        "s_mul_i32 s50, s40, 0x1000\n"
        "ds_read_b128 v[192:195], v12 offset:64\n"
        "v_mfma_f32_16x16x32_bf16 a[228:231], a[192:195], v[228:231], a[228:231]\n"
        "s_add_u32 m0, s24, 0x18400\n"
        "s_add_u32 s4, s25, s53\n"
        "ds_read_b128 v[196:199], v12 offset:576\n"
        "v_bfe_u32 v26, v1, 1, 2\n"
        "v_mfma_f32_16x16x32_bf16 a[232:235], a[192:195], v[232:235], a[232:235]\n"
        "ds_read_b128 v[200:203], v12 offset:1088\n"
        "v_lshrrev_b32 v25, 3, v1\n"
        "v_mfma_f32_16x16x32_bf16 a[236:239], a[192:195], v[236:239], a[236:239]\n"
        "global_load_lds_dwordx4 v18, s[14:15]\n"
        "s_add_u32 s53, s53, 0x80000\n"
        "ds_read_b128 v[204:207], v12 offset:1600\n"
        "v_lshl_add_u32 v25, v26, 2, v25\n"
        "v_mfma_f32_16x16x32_bf16 a[240:243], a[192:195], v[240:243], a[240:243]\n"
        "v_and_b32 v25, 15, v25\n"
        "v_mfma_f32_16x16x32_bf16 a[244:247], a[192:195], v[244:247], a[244:247]\n"
        "s_add_u32 m0, s24, 0x1c400\n"
        "s_and_b32 s53, s53, 0x3fffff\n"
        "ds_read_b128 v[208:211], v12 offset:2112\n"
        "v_lshl_or_b32 v25, v26, 4, v25\n"
        "v_mfma_f32_16x16x32_bf16 a[248:251], a[192:195], v[248:251], a[248:251]\n"
        "ds_read_b128 v[212:215], v12 offset:2624\n"
        "v_mfma_f32_16x16x32_bf16 a[252:255], a[192:195], v[252:255], a[252:255]\n"
        "global_load_lds_dwordx4 v18, s[16:17]\n"
        "s_add_u32 s21, s25, s53\n"
        "ds_read_b128 v[216:219], v12 offset:3136\n"
        "v_and_b32 v26, 1, v1\n"
        "v_mfma_f32_16x16x32_bf16 a[228:231], a[196:199], v[224:227], a[228:231]\n"
        "s_add_u32 s53, s53, 0x80000\n"
        "ds_read_b128 v[220:223], v12 offset:3648\n"
        "v_lshl_or_b32 v25, v25, 1, v26\n"
        "v_mfma_f32_16x16x32_bf16 a[232:235], a[196:199], v[228:231], a[232:235]\n"
        "s_add_u32 m0, s24, 0x14800\n"
        "v_lshl_add_u32 v11, v25, 3, s49\n"
        "v_mfma_f32_16x16x32_bf16 a[236:239], a[196:199], v[232:235], a[236:239]\n"
        "s_and_b32 s53, s53, 0x3fffff\n"
        "ds_read2_b32 a[160:161], v36 offset0:215 offset1:216\n"
        "v_add_u32 v11, 0x20000, v11\n"
        "v_mfma_f32_16x16x32_bf16 a[240:243], a[196:199], v[236:239], a[240:243]\n"
        "global_load_lds_dwordx4 v19, s[12:13]\n"
        "ds_read2_b32 a[162:163], v36 offset0:217 offset1:218\n"
        "v_xor_b32 v23, 0x80, v11\n"
        "v_mfma_f32_16x16x32_bf16 a[244:247], a[196:199], v[240:243], a[244:247]\n"
        "s_add_u32 s6, s25, s53\n"
        "ds_read2_b32 a[164:165], v36 offset0:87 offset1:88\n"
        "v_mfma_f32_16x16x32_bf16 a[248:251], a[196:199], v[244:247], a[248:251]\n"
        "s_add_u32 m0, s24, 0x18800\n"
        "ds_read2_b32 a[166:167], v36 offset0:89 offset1:90\n"
        "v_mfma_f32_16x16x32_bf16 a[252:255], a[196:199], v[248:251], a[252:255]\n"
        "s_add_u32 s53, s53, 0x80000\n"
        "v_mfma_f32_16x16x32_bf16 a[232:235], a[200:203], v[224:227], a[232:235]\n"
        "global_load_lds_dwordx4 v19, s[14:15]\n"
        "ds_read2_b32 a[168:169], v37 offset0:215 offset1:216\n"
        "v_mfma_f32_16x16x32_bf16 a[236:239], a[200:203], v[228:231], a[236:239]\n"
        "s_and_b32 s53, s53, 0x3fffff\n"
        "ds_read2_b32 a[170:171], v37 offset0:217 offset1:218\n"
        "v_mfma_f32_16x16x32_bf16 a[240:243], a[200:203], v[232:235], a[240:243]\n"
        "s_add_u32 m0, s24, 0x1c800\n"
        "s_add_u32 s23, s25, s53\n"
        "ds_read2_b32 a[172:173], v37 offset0:87 offset1:88\n"
        "v_mfma_f32_16x16x32_bf16 a[244:247], a[200:203], v[236:239], a[244:247]\n"
        "ds_read2_b32 a[174:175], v37 offset0:89 offset1:90\n"
        "v_mfma_f32_16x16x32_bf16 a[248:251], a[200:203], v[240:243], a[248:251]\n"
        "global_load_lds_dwordx4 v19, s[16:17]\n"
        "s_add_u32 s53, s53, 0x80000\n"
        "v_mfma_f32_16x16x32_bf16 a[252:255], a[200:203], v[244:247], a[252:255]\n"
        "ds_read2_b32 a[176:177], v38 offset0:215 offset1:216\n"
        "v_mfma_f32_16x16x32_bf16 a[236:239], a[204:207], v[224:227], a[236:239]\n"
        "s_add_u32 m0, s24, 0x14c00\n"
        "s_and_b32 s53, s53, 0x3fffff\n"
        "ds_read2_b32 a[178:179], v38 offset0:217 offset1:218\n"
        "v_mfma_f32_16x16x32_bf16 a[240:243], a[204:207], v[228:231], a[240:243]\n"
        "ds_read2_b32 a[180:181], v38 offset0:87 offset1:88\n"
        "v_mfma_f32_16x16x32_bf16 a[244:247], a[204:207], v[232:235], a[244:247]\n"
        "global_load_lds_dwordx4 v20, s[12:13]\n"
        "s_add_u32 s22, s25, s53\n"
        "ds_read2_b32 a[182:183], v38 offset0:89 offset1:90\n"
        "v_mfma_f32_16x16x32_bf16 a[248:251], a[204:207], v[236:239], a[248:251]\n"
        "v_mfma_f32_16x16x32_bf16 a[252:255], a[204:207], v[240:243], a[252:255]\n"
        "s_add_u32 m0, s24, 0x18c00\n"
        "s_add_u32 s53, s53, 0x80000\n"
        "ds_read2_b32 a[184:185], v39 offset0:215 offset1:216\n"
        "v_mfma_f32_16x16x32_bf16 a[240:243], a[208:211], v[224:227], a[240:243]\n"
        "s_and_b32 s53, s53, 0x3fffff\n"
        "ds_read2_b32 a[186:187], v39 offset0:217 offset1:218\n"
        "v_mfma_f32_16x16x32_bf16 a[244:247], a[208:211], v[228:231], a[244:247]\n"
        "global_load_lds_dwordx4 v20, s[14:15]\n"
        "ds_read2_b32 a[188:189], v39 offset0:87 offset1:88\n"
        "v_mfma_f32_16x16x32_bf16 a[248:251], a[208:211], v[232:235], a[248:251]\n"
        "s_add_u32 s18, s25, s53\n"
        "ds_read2_b32 a[190:191], v39 offset0:89 offset1:90\n"
        "v_mfma_f32_16x16x32_bf16 a[252:255], a[208:211], v[236:239], a[252:255]\n"
        "s_add_u32 m0, s24, 0x1cc00\n"
        "v_mfma_f32_16x16x32_bf16 a[244:247], a[212:215], v[224:227], a[244:247]\n"
        "s_lshl_b32 s25, s52, 8\n"
        "v_mfma_f32_16x16x32_bf16 a[248:251], a[212:215], v[228:231], a[248:251]\n"
        "global_load_lds_dwordx4 v20, s[16:17]\n"
        "v_mfma_f32_16x16x32_bf16 a[252:255], a[212:215], v[232:235], a[252:255]\n"
        "s_waitcnt vmcnt(8)\n"
        "v_cmp_ne_u32 vcc, s41, v99\n"
        "s_lshl_b32 s53, s44, 14\n"
        "v_mfma_f32_16x16x32_bf16 a[248:251], a[216:219], v[224:227], a[248:251]\n"
        "s_add_u32 s25, s25, s53\n"
        "v_mfma_f32_16x16x32_bf16 a[252:255], a[216:219], v[228:231], a[252:255]\n"
        "v_mfma_f32_16x16x32_bf16 a[252:255], a[220:223], v[224:227], a[252:255]\n"
        "s_cbranch_vccnz .LXW_RETRY_%=\n"
        ".LXW_DONE_%=:\n"
        "s_add_u32 s5, s25, 0x40000\n"
        "s_waitcnt lgkmcnt(0)\n"
        "s_barrier\n"
        "v_mfma_f32_16x16x32_bf16 a[224:227], a[160:163], v[192:195], a[224:227]\n"
        "ds_read_b128 v[224:227], v12\n"
        "v_mov_b32 v24, 0x3f803f80\n"
        "s_cbranch_execz .LWARM_RET_%=\n"
        "v_mfma_f32_16x16x32_bf16 a[228:231], a[160:163], v[196:199], a[228:231]\n"
        "buffer_load_dwordx4 a[0:3], v4, s[36:39], s92 offen sc1\n"
        "v_mov_b32 v27, s44\n"
        "s_mul_i32 s52, s52, 0x70\n"
        "ds_read_b128 v[228:231], v12 offset:512\n"
        "v_mfma_f32_16x16x32_bf16 a[232:235], a[160:163], v[200:203], a[232:235]\n"
        "ds_read_b128 v[232:235], v12 offset:1024\n"
        "v_and_b32 v27, 1, v27\n"
        "v_mfma_f32_16x16x32_bf16 a[236:239], a[160:163], v[204:207], a[236:239]\n"
        "v_add_u32 v28, s50, v5\n"
        "s_lshr_b32 s53, s44, 2\n"
        "ds_read_b128 v[236:239], v12 offset:1536\n"
        "v_mfma_f32_16x16x32_bf16 a[240:243], a[160:163], v[208:211], a[240:243]\n"
        "v_cmp_eq_u32 vcc, 0, v2\n"
        "v_mfma_f32_16x16x32_bf16 a[244:247], a[160:163], v[212:215], a[244:247]\n"
        "v_add_u32 v25, 0, v27\n"
        "s_mul_i32 s53, s53, 14\n"
        "ds_read_b128 v[240:243], v12 offset:2048\n"
        "v_mfma_f32_16x16x32_bf16 a[248:251], a[160:163], v[216:219], a[248:251]\n"
        "v_xor_b32 v25, v25, v6\n"
        "s_add_u32 s52, s52, s53\n"
        "ds_read_b128 v[244:247], v12 offset:2560\n"
        "v_mfma_f32_16x16x32_bf16 a[252:255], a[160:163], v[220:223], a[252:255]\n"
        "buffer_load_dwordx4 a[4:7], v4, s[36:39], s92 offen offset:1024 sc1\n"
        "ds_read_b128 v[248:251], v12 offset:3072\n"
        "v_mfma_f32_16x16x32_bf16 a[228:231], a[164:167], v[192:195], a[228:231]\n"
        "v_lshl_add_u32 v92, v25, 5, v28\n"
        "s_and_b32 s53, s44, 3\n"
        "ds_read_b128 v[252:255], v12 offset:3584\n"
        "v_mfma_f32_16x16x32_bf16 a[232:235], a[164:167], v[196:199], a[232:235]\n"
        "v_add_u32 v182, 0x10000, v92\n"
        "v_mfma_f32_16x16x32_bf16 a[236:239], a[164:167], v[200:203], a[236:239]\n"
        "v_cndmask_b32 v64, 0, v24, vcc\n"
        "s_add_u32 s62, s52, s53\n"
        "ds_read2_b32 a[192:193], v36 offset0:199 offset1:200\n"
        "v_mfma_f32_16x16x32_bf16 a[240:243], a[164:167], v[204:207], a[240:243]\n"
        "ds_read2_b32 a[194:195], v36 offset0:201 offset1:202\n"
        "v_cndmask_b32 v65, 0, v24, vcc\n"
        "v_mfma_f32_16x16x32_bf16 a[244:247], a[164:167], v[208:211], a[244:247]\n"
        "v_mov_b64 v[66:67], v[64:65]\n"
        "s_lshl_b32 s62, s62, 10\n"
        "ds_read2_b32 a[196:197], v36 offset0:71 offset1:72\n"
        "v_mfma_f32_16x16x32_bf16 a[248:251], a[164:167], v[212:215], a[248:251]\n"
        "v_cmp_eq_u32 vcc, 4, v2\n"
        "s_add_u32 s62, s62, 0x800000\n"
        "ds_read2_b32 a[198:199], v36 offset0:73 offset1:74\n"
        "v_mfma_f32_16x16x32_bf16 a[252:255], a[164:167], v[216:219], a[252:255]\n"
        "buffer_load_dwordx4 a[8:11], v4, s[36:39], s92 offen offset:2048 sc1\n"
        "v_add_u32 v25, 1, v27\n"
        "v_mfma_f32_16x16x32_bf16 a[232:235], a[168:171], v[192:195], a[232:235]\n"
        "s_bfe_u32 s53, s44, 0x10001\n"
        "ds_read2_b32 a[200:201], v37 offset0:199 offset1:200\n"
        "v_mfma_f32_16x16x32_bf16 a[236:239], a[168:171], v[196:199], a[236:239]\n"
        "ds_read2_b32 a[202:203], v37 offset0:201 offset1:202\n"
        "v_xor_b32 v25, v25, v6\n"
        "v_mfma_f32_16x16x32_bf16 a[240:243], a[168:171], v[200:203], a[240:243]\n"
        "v_lshl_add_u32 v93, v25, 5, v28\n"
        "s_mul_i32 s53, s53, 56\n"
        "ds_read2_b32 a[204:205], v37 offset0:71 offset1:72\n"
        "v_mfma_f32_16x16x32_bf16 a[244:247], a[168:171], v[204:207], a[244:247]\n"
        "ds_read2_b32 a[206:207], v37 offset0:73 offset1:74\n"
        "v_add_u32 v183, 0x10000, v93\n"
        "v_mfma_f32_16x16x32_bf16 a[248:251], a[168:171], v[208:211], a[248:251]\n"
        "v_cndmask_b32 v68, 0, v24, vcc\n"
        "s_add_u32 s52, s52, s53\n"
        "v_mfma_f32_16x16x32_bf16 a[252:255], a[168:171], v[212:215], a[252:255]\n"
        "buffer_load_dwordx4 a[12:15], v4, s[36:39], s92 offen offset:3072 sc1\n"
        "ds_read2_b32 a[208:209], v38 offset0:199 offset1:200\n"
        "v_cndmask_b32 v69, 0, v24, vcc\n"
        "v_mfma_f32_16x16x32_bf16 a[236:239], a[172:175], v[192:195], a[236:239]\n"
        "v_mov_b64 v[70:71], v[68:69]\n"
        "s_and_b32 s53, s44, 1\n"
        "ds_read2_b32 a[210:211], v38 offset0:201 offset1:202\n"
        "v_mfma_f32_16x16x32_bf16 a[240:243], a[172:175], v[196:199], a[240:243]\n"
        "s_add_u32 s52, s52, s53\n"
        "ds_read2_b32 a[212:213], v38 offset0:71 offset1:72\n"
        "v_mfma_f32_16x16x32_bf16 a[244:247], a[172:175], v[200:203], a[244:247]\n"
        "ds_read2_b32 a[214:215], v38 offset0:73 offset1:74\n"
        "v_cmp_eq_u32 vcc, 8, v2\n"
        "v_mfma_f32_16x16x32_bf16 a[248:251], a[172:175], v[204:207], a[248:251]\n"
        "v_add_u32 v25, 2, v27\n"
        "s_add_u32 s52, s52, 12\n"
        "v_mfma_f32_16x16x32_bf16 a[252:255], a[172:175], v[208:211], a[252:255]\n"
        "ds_read2_b32 a[216:217], v39 offset0:199 offset1:200\n"
        "v_xor_b32 v25, v25, v6\n"
        "v_mfma_f32_16x16x32_bf16 a[240:243], a[176:179], v[192:195], a[240:243]\n"
        "v_lshl_add_u32 v94, v25, 5, v28\n"
        "s_lshl_b32 s52, s52, 10\n"
        "ds_read2_b32 a[218:219], v39 offset0:201 offset1:202\n"
        "v_mfma_f32_16x16x32_bf16 a[244:247], a[176:179], v[196:199], a[244:247]\n"
        "buffer_load_dwordx4 a[64:67], v4, s[36:39], s26 offen sc1\n"
        "ds_read2_b32 a[220:221], v39 offset0:71 offset1:72\n"
        "v_add_u32 v184, 0x10000, v94\n"
        "v_mfma_f32_16x16x32_bf16 a[248:251], a[176:179], v[200:203], a[248:251]\n"
        "v_cndmask_b32 v72, 0, v24, vcc\n"
        "s_add_u32 s3, s52, 0x800000\n"
        "ds_read2_b32 a[222:223], v39 offset0:73 offset1:74\n"
        "v_mfma_f32_16x16x32_bf16 a[252:255], a[176:179], v[204:207], a[252:255]\n"
        "v_cndmask_b32 v73, 0, v24, vcc\n"
        "v_mfma_f32_16x16x32_bf16 a[244:247], a[180:183], v[192:195], a[244:247]\n"
        "s_mov_b32 s54, 0xff00ff\n"
        "v_mfma_f32_16x16x32_bf16 a[248:251], a[180:183], v[196:199], a[248:251]\n"
        "v_mov_b64 v[74:75], v[72:73]\n"
        "s_mov_b32 s55, 0xff00ff\n"
        "v_mfma_f32_16x16x32_bf16 a[252:255], a[180:183], v[200:203], a[252:255]\n"
        "v_cmp_eq_u32 vcc, 12, v2\n"
        "v_mfma_f32_16x16x32_bf16 a[248:251], a[184:187], v[192:195], a[248:251]\n"
        "v_add_u32 v25, 3, v27\n"
        "s_mov_b32 s56, 0xff00ff00\n"
        "v_mfma_f32_16x16x32_bf16 a[252:255], a[184:187], v[196:199], a[252:255]\n"
        "buffer_load_dwordx4 a[16:19], v4, s[36:39], s93 offen sc1\n"
        "v_xor_b32 v25, v25, v6\n"
        "v_mfma_f32_16x16x32_bf16 a[252:255], a[188:191], v[192:195], a[252:255]\n"
        "v_lshl_add_u32 v95, v25, 5, v28\n"
        "s_mov_b32 s57, 0xff00ff00\n"
        "s_waitcnt lgkmcnt(0)\n"
        "s_barrier\n"
        "v_mfma_f32_16x16x32_bf16 a[224:227], a[192:195], v[224:227], a[224:227]\n"
        "v_add_u32 v185, 0x10000, v95\n"
        "v_mfma_f32_16x16x32_bf16 a[228:231], a[192:195], v[228:231], a[228:231]\n"
        "v_cndmask_b32 v76, 0, v24, vcc\n"
        "s_and_b32 s53, s40, 1\n"
        "v_mfma_f32_16x16x32_bf16 a[232:235], a[192:195], v[232:235], a[232:235]\n"
        "s_lshl1_add_u32 s52, s45, s42\n"
        "v_mfma_f32_16x16x32_bf16 a[236:239], a[192:195], v[236:239], a[236:239]\n"
        "v_cndmask_b32 v77, 0, v24, vcc\n"
        "v_mfma_f32_16x16x32_bf16 a[240:243], a[192:195], v[240:243], a[240:243]\n"
        "buffer_load_dwordx4 a[68:71], v4, s[36:39], s26 offen offset:1024 sc1\n"
        "v_mov_b64 v[78:79], v[76:77]\n"
        "s_lshl1_add_u32 s63, s46, s53\n"
        "v_mfma_f32_16x16x32_bf16 a[244:247], a[192:195], v[244:247], a[244:247]\n"
        "v_cmp_eq_u32 vcc, 1, v2\n"
        "v_mfma_f32_16x16x32_bf16 a[248:251], a[192:195], v[248:251], a[248:251]\n"
        "v_add_u32 v25, 4, v27\n"
        "s_lshl_b32 s63, s63, 11\n"
        "ds_write_b128 v10, a[224:227]\n"
        "v_mfma_f32_16x16x32_bf16 a[252:255], a[192:195], v[252:255], a[252:255]\n"
        "v_xor_b32 v25, v25, v6\n"
        "v_mfma_f32_16x16x32_bf16 a[228:231], a[196:199], v[224:227], a[228:231]\n"
        "v_lshl_add_u32 v96, v25, 5, v28\n"
        "s_add_u32 s63, s63, s52\n"
        "v_mfma_f32_16x16x32_bf16 a[232:235], a[196:199], v[228:231], a[232:235]\n"
        "v_mfma_f32_16x16x32_bf16 a[236:239], a[196:199], v[232:235], a[236:239]\n"
        "v_add_u32 v186, 0x10000, v96\n"
        "s_lshl_b32 s63, s63, 7\n"
        "v_mfma_f32_16x16x32_bf16 a[240:243], a[196:199], v[236:239], a[240:243]\n"
        "buffer_load_dwordx4 a[20:23], v4, s[36:39], s93 offen offset:1024 sc1\n"
        "v_cndmask_b32 v80, 0, v24, vcc\n"
        "s_add_u32 s63, s63, 0x400000\n"
        "v_mfma_f32_16x16x32_bf16 a[244:247], a[196:199], v[240:243], a[244:247]\n"
        "v_cndmask_b32 v81, 0, v24, vcc\n"
        "v_mfma_f32_16x16x32_bf16 a[248:251], a[196:199], v[244:247], a[248:251]\n"
        "v_mov_b64 v[82:83], v[80:81]\n"
        "s_mul_i32 s52, s45, 0xe0\n"
        "v_mfma_f32_16x16x32_bf16 a[252:255], a[196:199], v[248:251], a[252:255]\n"
        "ds_write_b128 v10, a[228:231] offset:1024\n"
        "v_cmp_eq_u32 vcc, 5, v2\n"
        "v_mfma_f32_16x16x32_bf16 a[232:235], a[200:203], v[224:227], a[232:235]\n"
        "v_add_u32 v25, 5, v27\n"
        "s_mul_i32 s53, s40, 14\n"
        "v_mfma_f32_16x16x32_bf16 a[236:239], a[200:203], v[228:231], a[236:239]\n"
        "v_xor_b32 v25, v25, v6\n"
        "v_mfma_f32_16x16x32_bf16 a[240:243], a[200:203], v[232:235], a[240:243]\n"
        "buffer_load_dwordx4 a[24:27], v4, s[36:39], s93 offen offset:2048 sc1\n"
        "s_add_u32 s52, s52, s53\n"
        "v_mfma_f32_16x16x32_bf16 a[244:247], a[200:203], v[236:239], a[244:247]\n"
        "v_lshl_add_u32 v97, v25, 5, v28\n"
        "s_lshl_b32 s52, s52, 10\n"
        "v_mfma_f32_16x16x32_bf16 a[248:251], a[200:203], v[240:243], a[248:251]\n"
        "v_add_u32 v187, 0x10000, v97\n"
        "v_mfma_f32_16x16x32_bf16 a[252:255], a[200:203], v[244:247], a[252:255]\n"
        "v_cndmask_b32 v84, 0, v24, vcc\n"
        "s_lshr_b32 s20, s46, 2\n"
        "v_mfma_f32_16x16x32_bf16 a[236:239], a[204:207], v[224:227], a[236:239]\n"
        "ds_write_b128 v10, a[232:235] offset:8192\n"
        "v_cndmask_b32 v85, 0, v24, vcc\n"
        "v_mfma_f32_16x16x32_bf16 a[240:243], a[204:207], v[228:231], a[240:243]\n"
        "v_mov_b64 v[86:87], v[84:85]\n"
        "s_mul_i32 s53, s20, 0x1c000\n"
        "v_mfma_f32_16x16x32_bf16 a[244:247], a[204:207], v[232:235], a[244:247]\n"
        "v_cmp_eq_u32 vcc, 9, v2\n"
        "v_mfma_f32_16x16x32_bf16 a[248:251], a[204:207], v[236:239], a[248:251]\n"
        "buffer_load_dwordx4 a[72:75], v4, s[36:39], s26 offen offset:2048 sc1\n"
        "v_add_u32 v25, 6, v27\n"
        "s_add_u32 s30, s52, s53\n"
        "v_mfma_f32_16x16x32_bf16 a[252:255], a[204:207], v[240:243], a[252:255]\n"
        "v_mfma_f32_16x16x32_bf16 a[240:243], a[208:211], v[224:227], a[240:243]\n"
        "v_xor_b32 v25, v25, v6\n"
        "s_sub_u32 s47, s52, s53\n"
        "v_mfma_f32_16x16x32_bf16 a[244:247], a[208:211], v[228:231], a[244:247]\n"
        "v_lshl_add_u32 v98, v25, 5, v28\n"
        "s_add_u32 s30, s30, 0x800000\n"
        "ds_write_b128 v10, a[236:239] offset:9216\n"
        "v_mfma_f32_16x16x32_bf16 a[248:251], a[208:211], v[232:235], a[248:251]\n"
        "v_add_u32 v188, 0x10000, v98\n"
        "v_mfma_f32_16x16x32_bf16 a[252:255], a[208:211], v[236:239], a[252:255]\n"
        "v_cndmask_b32 v88, 0, v24, vcc\n"
        "s_add_u32 s47, s47, 0x81c000\n"
        "v_mfma_f32_16x16x32_bf16 a[244:247], a[212:215], v[224:227], a[244:247]\n"
        "v_cndmask_b32 v89, 0, v24, vcc\n"
        "v_mfma_f32_16x16x32_bf16 a[248:251], a[212:215], v[228:231], a[248:251]\n"
        "v_mov_b64 v[90:91], v[88:89]\n"
        "s_add_u32 s31, s30, 0xe000\n"
        "v_mfma_f32_16x16x32_bf16 a[252:255], a[212:215], v[232:235], a[252:255]\n"
        "ds_write_b128 v10, a[240:243] offset:16384\n"
        "v_mfma_f32_16x16x32_bf16 a[248:251], a[216:219], v[224:227], a[248:251]\n"
        "s_add_u32 s48, s47, 0xe000\n"
        "v_mfma_f32_16x16x32_bf16 a[252:255], a[216:219], v[228:231], a[252:255]\n"
        "v_mfma_f32_16x16x32_bf16 a[252:255], a[220:223], v[224:227], a[252:255]\n"
        "ds_write_b128 v10, a[244:247] offset:17408\n"
        "s_nop 3\n"
        "ds_write_b128 v10, a[248:251] offset:24576\n"
        "s_nop 1\n"
        "ds_write_b128 v10, a[252:255] offset:25600\n"
        "s_waitcnt lgkmcnt(0)\n"
        "s_barrier\n"
        "ds_read_b64 v[110:111], v11\n"
        "ds_read_b64 v[112:113], v11 offset:2048\n"
        "ds_read_b64 v[114:115], v11 offset:4096\n"
        "ds_read_b64 v[116:117], v11 offset:6144\n"
        "ds_read_b64 v[118:119], v23\n"
        "ds_read_b64 v[120:121], v23 offset:2048\n"
        "ds_read_b64 v[122:123], v23 offset:4096\n"
        "ds_read_b64 v[124:125], v23 offset:6144\n"
        "ds_read_b64 v[126:127], v11 offset:1024\n"
        "ds_read_b64 v[128:129], v11 offset:3072\n"
        "ds_read_b64 v[130:131], v11 offset:5120\n"
        "ds_read_b64 v[132:133], v11 offset:7168\n"
        "ds_read_b64 v[134:135], v23 offset:1024\n"
        "ds_read_b64 v[136:137], v23 offset:3072\n"
        "ds_read_b64 v[138:139], v23 offset:5120\n"
        "ds_read_b64 v[140:141], v23 offset:7168\n"
        "s_mov_b32 s49, 0xfffeffff\n"
        "s_waitcnt lgkmcnt(12)\n"
        "v_pk_add_f32 v[48:49], v[48:49], v[110:111]\n"
        "v_pk_add_f32 v[56:57], v[56:57], v[114:115]\n"
        "v_pk_add_f32 v[48:49], v[48:49], v[112:113]\n"
        "v_pk_add_f32 v[56:57], v[56:57], v[116:117]\n"
        "v_cvt_pk_bf16_f32 v142, v48, v56\n"
        "v_cvt_pk_bf16_f32 v143, v49, v57\n"
        "v_min_u32 v143, s49, v143\n"
        "buffer_store_dwordx2 v[142:143], v7, s[36:39], s98 offen sc1\n"
        "s_waitcnt lgkmcnt(8)\n"
        "v_pk_add_f32 v[50:51], v[50:51], v[118:119]\n"
        "v_pk_add_f32 v[58:59], v[58:59], v[122:123]\n"
        "v_pk_add_f32 v[50:51], v[50:51], v[120:121]\n"
        "v_pk_add_f32 v[58:59], v[58:59], v[124:125]\n"
        "v_cvt_pk_bf16_f32 v144, v50, v58\n"
        "v_cvt_pk_bf16_f32 v145, v51, v59\n"
        "v_min_u32 v145, s49, v145\n"
        "buffer_store_dwordx2 v[144:145], v7, s[36:39], s98 offen offset:512 sc1\n"
        "s_waitcnt lgkmcnt(4)\n"
        "v_pk_add_f32 v[52:53], v[52:53], v[126:127]\n"
        "v_pk_add_f32 v[60:61], v[60:61], v[130:131]\n"
        "v_pk_add_f32 v[52:53], v[52:53], v[128:129]\n"
        "v_pk_add_f32 v[60:61], v[60:61], v[132:133]\n"
        "v_cvt_pk_bf16_f32 v146, v52, v60\n"
        "v_cvt_pk_bf16_f32 v147, v53, v61\n"
        "v_min_u32 v147, s49, v147\n"
        "buffer_store_dwordx2 v[146:147], v7, s[36:39], s98 offen offset:1024 sc1\n"
        "s_waitcnt lgkmcnt(0)\n"
        "v_pk_add_f32 v[54:55], v[54:55], v[134:135]\n"
        "v_pk_add_f32 v[62:63], v[62:63], v[138:139]\n"
        "v_pk_add_f32 v[54:55], v[54:55], v[136:137]\n"
        "v_pk_add_f32 v[62:63], v[62:63], v[140:141]\n"
        "v_cvt_pk_bf16_f32 v148, v54, v62\n"
        "v_cvt_pk_bf16_f32 v149, v55, v63\n"
        "v_min_u32 v149, s49, v149\n"
        "buffer_store_dwordx2 v[148:149], v7, s[36:39], s98 offen offset:1536 sc1\n"
        "s_lshl_b32 s49, s40, 12\n"
        "buffer_load_dwordx4 a[28:31], v4, s[36:39], s93 offen offset:3072 sc1\n"
        "v_subrev_u32 v13, s49, v92\n"
        "v_subrev_u32 v14, s49, v93\n"
        "buffer_load_dwordx4 a[76:79], v4, s[36:39], s26 offen offset:3072 sc1\n"
        "v_subrev_u32 v15, s49, v94\n"
        "buffer_load_dwordx4 a[32:35], v4, s[36:39], s94 offen sc1\n"
        "v_subrev_u32 v16, s49, v95\n"
        "v_subrev_u32 v17, s49, v96\n"
        "buffer_load_dwordx4 a[36:39], v4, s[36:39], s94 offen offset:1024 sc1\n"
        "v_subrev_u32 v18, s49, v97\n"
        "v_subrev_u32 v19, s49, v98\n"
        "buffer_load_dwordx4 a[80:83], v4, s[36:39], s27 offen sc1\n"
        "v_add_u32 v42, 0x10000, v13\n"
        "v_add_u32 v43, 0x10000, v14\n"
        "buffer_load_dwordx4 a[40:43], v4, s[36:39], s94 offen offset:2048 sc1\n"
        "v_add_u32 v44, 0x10000, v19\n"
        "v_lshrrev_b32 v0, 3, v1\n"
        "buffer_load_dwordx4 a[84:87], v4, s[36:39], s27 offen offset:1024 sc1\n"
        "v_and_b32 v25, 7, v1\n"
        "buffer_load_dwordx4 a[44:47], v4, s[36:39], s94 offen offset:3072 sc1\n"
        "v_lshlrev_b32 v0, 14, v0\n"
        "v_lshl_add_u32 v0, v25, 4, v0\n"
        "buffer_load_dwordx4 a[48:51], v4, s[36:39], s95 offen sc1\n"
        "v_add_u32 v20, 0x20000, v0\n"
        "v_add_u32 v40, 0x20000, v20\n"
        "buffer_load_dwordx4 a[88:91], v4, s[36:39], s27 offen offset:2048 sc1\n"
        "v_add_u32 v41, 0x20000, v40\n"
        "s_waitcnt vmcnt(24)\n"
        "ds_read_b64_tr_b16 a[224:225], v92\n"
        "ds_read_b64_tr_b16 a[226:227], v182\n"
        "ds_read_b64_tr_b16 a[228:229], v93\n"
        "ds_read_b64_tr_b16 a[230:231], v183\n"
        "ds_read_b64_tr_b16 a[232:233], v94\n"
        "ds_read_b64_tr_b16 a[234:235], v184\n"
        "ds_read_b64_tr_b16 a[236:237], v95\n"
        "ds_read_b64_tr_b16 a[238:239], v185\n"
        "ds_read_b64_tr_b16 a[240:241], v96\n"
        "ds_read_b64_tr_b16 a[242:243], v186\n"
        "ds_read_b64_tr_b16 a[244:245], v97\n"
        "ds_read_b64_tr_b16 a[246:247], v187\n"
        "ds_read_b64_tr_b16 a[248:249], v98\n"
        "ds_read_b64_tr_b16 a[250:251], v188\n"
        "s_waitcnt lgkmcnt(12)\n"
        "v_mfma_f32_16x16x32_bf16 v[106:109], v[64:67], a[224:227], 0\n"
        "ds_read_b64_tr_b16 a[224:225], v92 offset:16384\n"
        "ds_read_b64_tr_b16 a[226:227], v182 offset:16384\n"
        "s_waitcnt lgkmcnt(12)\n"
        "v_mfma_f32_16x16x32_bf16 v[106:109], v[68:71], a[228:231], v[106:109]\n"
        "ds_read_b64_tr_b16 a[228:229], v93 offset:16384\n"
        "ds_read_b64_tr_b16 a[230:231], v183 offset:16384\n"
        "s_waitcnt lgkmcnt(12)\n"
        "v_mfma_f32_16x16x32_bf16 v[106:109], v[72:75], a[232:235], v[106:109]\n"
        "ds_read_b64_tr_b16 a[232:233], v94 offset:16384\n"
        "ds_read_b64_tr_b16 a[234:235], v184 offset:16384\n"
        "s_waitcnt lgkmcnt(12)\n"
        "v_mfma_f32_16x16x32_bf16 v[106:109], v[76:79], a[236:239], v[106:109]\n"
        "ds_read_b64_tr_b16 a[236:237], v95 offset:16384\n"
        "ds_read_b64_tr_b16 a[238:239], v185 offset:16384\n"
        "s_waitcnt lgkmcnt(12)\n"
        "v_mfma_f32_16x16x32_bf16 v[106:109], v[80:83], a[240:243], v[106:109]\n"
        "ds_read_b64_tr_b16 a[240:241], v96 offset:16384\n"
        "ds_read_b64_tr_b16 a[242:243], v186 offset:16384\n"
        "s_waitcnt lgkmcnt(12)\n"
        "v_mfma_f32_16x16x32_bf16 v[106:109], v[84:87], a[244:247], v[106:109]\n"
        "ds_read_b64_tr_b16 a[244:245], v97 offset:16384\n"
        "ds_read_b64_tr_b16 a[246:247], v187 offset:16384\n"
        "s_waitcnt lgkmcnt(12)\n"
        "v_mfma_f32_16x16x32_bf16 v[106:109], v[88:91], a[248:251], v[106:109]\n"
        "ds_read_b64_tr_b16 a[248:249], v98 offset:16384\n"
        "ds_read_b64_tr_b16 a[250:251], v188 offset:16384\n"
        "s_waitcnt lgkmcnt(12)\n"
        "v_mfma_f32_16x16x32_bf16 v[106:109], v[64:67], a[224:227], v[106:109]\n"
        "ds_read_b64_tr_b16 a[224:225], v92 offset:32768\n"
        "ds_read_b64_tr_b16 a[226:227], v182 offset:32768\n"
        "s_waitcnt lgkmcnt(12)\n"
        "v_mfma_f32_16x16x32_bf16 v[106:109], v[68:71], a[228:231], v[106:109]\n"
        "ds_read_b64_tr_b16 a[228:229], v93 offset:32768\n"
        "ds_read_b64_tr_b16 a[230:231], v183 offset:32768\n"
        "s_waitcnt lgkmcnt(12)\n"
        "v_mfma_f32_16x16x32_bf16 v[106:109], v[72:75], a[232:235], v[106:109]\n"
        "ds_read_b64_tr_b16 a[232:233], v94 offset:32768\n"
        "ds_read_b64_tr_b16 a[234:235], v184 offset:32768\n"
        "s_waitcnt lgkmcnt(12)\n"
        "v_mfma_f32_16x16x32_bf16 v[106:109], v[76:79], a[236:239], v[106:109]\n"
        "ds_read_b64_tr_b16 a[236:237], v95 offset:32768\n"
        "ds_read_b64_tr_b16 a[238:239], v185 offset:32768\n"
        "s_waitcnt lgkmcnt(12)\n"
        "v_mfma_f32_16x16x32_bf16 v[106:109], v[80:83], a[240:243], v[106:109]\n"
        "ds_read_b64_tr_b16 a[240:241], v96 offset:32768\n"
        "ds_read_b64_tr_b16 a[242:243], v186 offset:32768\n"
        "s_waitcnt lgkmcnt(12)\n"
        "v_mfma_f32_16x16x32_bf16 v[106:109], v[84:87], a[244:247], v[106:109]\n"
        "ds_read_b64_tr_b16 a[244:245], v97 offset:32768\n"
        "ds_read_b64_tr_b16 a[246:247], v187 offset:32768\n"
        "s_waitcnt lgkmcnt(12)\n"
        "v_mfma_f32_16x16x32_bf16 v[106:109], v[88:91], a[248:251], v[106:109]\n"
        "ds_read_b64_tr_b16 a[248:249], v98 offset:32768\n"
        "ds_read_b64_tr_b16 a[250:251], v188 offset:32768\n"
        "s_waitcnt lgkmcnt(12)\n"
        "v_mfma_f32_16x16x32_bf16 v[106:109], v[64:67], a[224:227], v[106:109]\n"
        "ds_read_b64_tr_b16 a[224:225], v92 offset:49152\n"
        "ds_read_b64_tr_b16 a[226:227], v182 offset:49152\n"
        "s_waitcnt lgkmcnt(12)\n"
        "v_mfma_f32_16x16x32_bf16 v[106:109], v[68:71], a[228:231], v[106:109]\n"
        "ds_read_b64_tr_b16 a[228:229], v93 offset:49152\n"
        "ds_read_b64_tr_b16 a[230:231], v183 offset:49152\n"
        "s_waitcnt lgkmcnt(12)\n"
        "v_mfma_f32_16x16x32_bf16 v[106:109], v[72:75], a[232:235], v[106:109]\n"
        "ds_read_b64_tr_b16 a[232:233], v94 offset:49152\n"
        "ds_read_b64_tr_b16 a[234:235], v184 offset:49152\n"
        "s_waitcnt lgkmcnt(12)\n"
        "v_mfma_f32_16x16x32_bf16 v[106:109], v[76:79], a[236:239], v[106:109]\n"
        "ds_read_b64_tr_b16 a[236:237], v95 offset:49152\n"
        "ds_read_b64_tr_b16 a[238:239], v185 offset:49152\n"
        "s_waitcnt lgkmcnt(12)\n"
        "v_mfma_f32_16x16x32_bf16 v[106:109], v[80:83], a[240:243], v[106:109]\n"
        "ds_read_b64_tr_b16 a[240:241], v96 offset:49152\n"
        "ds_read_b64_tr_b16 a[242:243], v186 offset:49152\n"
        "s_waitcnt lgkmcnt(12)\n"
        "v_mfma_f32_16x16x32_bf16 v[106:109], v[84:87], a[244:247], v[106:109]\n"
        "ds_read_b64_tr_b16 a[244:245], v97 offset:49152\n"
        "ds_read_b64_tr_b16 a[246:247], v187 offset:49152\n"
        "s_waitcnt lgkmcnt(12)\n"
        "v_mfma_f32_16x16x32_bf16 v[106:109], v[88:91], a[248:251], v[106:109]\n"
        "s_waitcnt vmcnt(10)\n"
        "buffer_load_dwordx4 v[128:131], v0, s[36:39], s8 offen sc1\n"
        "ds_read_b64_tr_b16 a[248:249], v98 offset:49152\n"
        "ds_read_b64_tr_b16 a[250:251], v188 offset:49152\n"
        "s_waitcnt lgkmcnt(12)\n"
        "v_mfma_f32_16x16x32_bf16 v[106:109], v[64:67], a[224:227], v[106:109]\n"
        "s_waitcnt lgkmcnt(10)\n"
        "v_mfma_f32_16x16x32_bf16 v[106:109], v[68:71], a[228:231], v[106:109]\n"
        "buffer_load_dwordx4 v[132:135], v20, s[36:39], s8 offen sc1\n"
        "s_waitcnt lgkmcnt(8)\n"
        "v_mfma_f32_16x16x32_bf16 v[106:109], v[72:75], a[232:235], v[106:109]\n"
        "s_waitcnt lgkmcnt(6)\n"
        "v_mfma_f32_16x16x32_bf16 v[106:109], v[76:79], a[236:239], v[106:109]\n"
        "buffer_load_dwordx4 v[136:139], v40, s[36:39], s8 offen sc1\n"
        "s_waitcnt lgkmcnt(4)\n"
        "v_mfma_f32_16x16x32_bf16 v[106:109], v[80:83], a[240:243], v[106:109]\n"
        "s_waitcnt lgkmcnt(2)\n"
        "v_mfma_f32_16x16x32_bf16 v[106:109], v[84:87], a[244:247], v[106:109]\n"
        "buffer_load_dwordx4 v[140:143], v41, s[36:39], s8 offen sc1\n"
        "s_waitcnt lgkmcnt(0)\n"
        "v_mfma_f32_16x16x32_bf16 v[106:109], v[88:91], a[248:251], v[106:109]\n"
        "s_lshl_b32 s49, s40, 13\n"
        "s_add_u32 s49, s49, 0x21000\n"
        "v_add_u32 v24, s49, v4\n"
        "s_nop 4\n"
        "ds_write_b64 v24, v[106:107]\n"
        "s_waitcnt lgkmcnt(0)\n"
        "s_barrier\n"
        "v_add_u32 v24, 0x21000, v4\n"
        "ds_read_b64 v[48:49], v24\n"
        "ds_read_b64 v[50:51], v24 offset:8192\n"
        "buffer_load_dwordx4 v[144:147], v0, s[36:39], s9 offen sc1\n"
        "ds_read_b64 v[52:53], v24 offset:16384\n"
        "ds_read_b64 v[54:55], v24 offset:24576\n"
        "v_mov_b32 v76, 0x3f803f80\n"
        "v_mov_b32 v77, 0x3f803f80\n"
        "v_mov_b32 v78, 0x3f803f80\n"
        "v_mov_b32 v79, 0x3f803f80\n"
        "buffer_load_dwordx4 v[148:151], v20, s[36:39], s9 offen sc1\n"
        "v_mov_b64 v[82:83], 0\n"
        "v_mov_b64 v[86:87], 0\n"
        "v_mov_b64 v[74:75], 0\n"
        "s_waitcnt lgkmcnt(0)\n"
        "v_pk_add_f32 v[48:49], v[48:49], v[50:51]\n"
        "v_pk_add_f32 v[52:53], v[52:53], v[54:55]\n"
        "buffer_load_dwordx4 v[152:155], v40, s[36:39], s9 offen sc1\n"
        "v_pk_add_f32 v[48:49], v[48:49], v[52:53]\n"
        "v_cvt_pk_bf16_f32 v26, v48, v48\n"
        "v_and_b32 v26, s1, v26\n"
        "v_sub_f32 v27, v48, v26\n"
        "v_cvt_pk_bf16_f32 v50, v26, v27\n"
        "v_cvt_pk_bf16_f32 v26, v49, v49\n"
        "buffer_load_dwordx4 v[156:159], v41, s[36:39], s9 offen sc1\n"
        "v_and_b32 v26, s1, v26\n"
        "v_sub_f32 v27, v49, v26\n"
        "v_cvt_pk_bf16_f32 v51, v26, v27\n"
        "v_cmp_eq_u32 vcc, 0, v3\n"
        "s_nop 1\n"
        "v_cndmask_b32 v88, 0, v50, vcc\n"
        "buffer_load_dwordx4 v[160:163], v0, s[36:39], s4 offen sc1\n"
        "v_cndmask_b32 v89, 0, v21, vcc\n"
        "v_cndmask_b32 v96, 0, v51, vcc\n"
        "v_cndmask_b32 v97, 0, v22, vcc\n"
        "v_cmp_eq_u32 vcc, 1, v3\n"
        "s_nop 1\n"
        "v_cndmask_b32 v90, 0, v50, vcc\n"
        "buffer_load_dwordx4 v[164:167], v20, s[36:39], s4 offen sc1\n"
        "v_cndmask_b32 v91, 0, v21, vcc\n"
        "v_cndmask_b32 v98, 0, v51, vcc\n"
        "v_cndmask_b32 v99, 0, v22, vcc\n"
        "v_cmp_eq_u32 vcc, 2, v3\n"
        "s_nop 1\n"
        "v_cndmask_b32 v92, 0, v50, vcc\n"
        "v_cndmask_b32 v93, 0, v21, vcc\n"
        "buffer_load_dwordx4 v[168:171], v40, s[36:39], s4 offen sc1\n"
        "v_cndmask_b32 v100, 0, v51, vcc\n"
        "v_cndmask_b32 v101, 0, v22, vcc\n"
        "v_cmp_eq_u32 vcc, 3, v3\n"
        "s_nop 1\n"
        "v_cndmask_b32 v94, 0, v50, vcc\n"
        "v_cndmask_b32 v95, 0, v21, vcc\n"
        "buffer_load_dwordx4 v[172:175], v41, s[36:39], s4 offen sc1\n"
        "s_mov_b32 s10, 0x100001\n"
        "s_mov_b32 s11, 0x10000100\n"
        "s_mov_b32 s12, 0x200002\n"
        "s_mov_b32 s13, 0x20000200\n"
        "s_mov_b32 s14, 0x400004\n"
        "s_mov_b32 s15, 0x40000400\n"
        "buffer_load_dwordx4 v[176:179], v0, s[36:39], s21 offen sc1\n"
        "s_mov_b32 s16, 0x800008\n"
        "s_mov_b32 s17, 0x80000800\n"
        "v_lshrrev_b32 v25, 5, v1\n"
        "v_lshl_add_u32 v25, v2, 1, v25\n"
        "v_lshl_add_u32 v11, v25, 3, s5\n"
        "s_lshl_b32 s50, s40, 13\n"
        "buffer_load_dwordx4 v[180:183], v20, s[36:39], s21 offen sc1\n"
        "s_add_u32 s50, s50, 0x20000\n"
        "v_xor_b32 v25, v1, v3\n"
        "v_lshl_add_u32 v12, v25, 4, s50\n"
        "s_sub_u32 s50, s92, s19\n"
        "s_lshr_b32 s50, s50, 2\n"
        "v_add_u32 v7, s50, v4\n"
        "buffer_load_dwordx4 v[184:187], v40, s[36:39], s21 offen sc1\n"
        "s_sub_u32 s50, s93, s19\n"
        "s_lshr_b32 s50, s50, 2\n"
        "v_add_u32 v8, s50, v4\n"
        "s_sub_u32 s50, s94, s19\n"
        "s_lshr_b32 s50, s50, 2\n"
        "v_add_u32 v9, s50, v4\n"
        "buffer_load_dwordx4 v[188:191], v41, s[36:39], s21 offen sc1\n"
        "s_sub_u32 s50, s95, s19\n"
        "s_lshr_b32 s50, s50, 2\n"
        "v_add_u32 v10, s50, v4\n"
        "ds_read_b64_tr_b16 a[224:225], v13\n"
        "ds_read_b64_tr_b16 a[226:227], v42\n"
        "ds_read_b64_tr_b16 a[228:229], v14\n"
        "ds_read_b64_tr_b16 a[230:231], v43\n"
        "ds_read_b64_tr_b16 a[232:233], v19\n"
        "ds_read_b64_tr_b16 a[234:235], v44\n"
        "s_waitcnt vmcnt(14)\n"
        "v_max3_u32 v24, v129, v131, v133\n"
        "v_max_u32 v24, v24, v135\n"
        "v_cmp_le_u32 vcc, s1, v24\n"
        "s_nop 0\n"
        "s_cbranch_vccnz .LX20_RETRY_%=\n"
        ".LX20_DONE_%=:\n"
        "s_mov_b64 vcc, s[54:55]\n"
        "s_nop 0\n"
        "v_cndmask_b32_dpp v120, v130, v128, vcc row_ror:8 row_mask:0xf bank_mask:0xf\n"
        "v_cndmask_b32_dpp v122, v134, v132, vcc row_ror:8 row_mask:0xf bank_mask:0xf\n"
        "v_cndmask_b32_dpp v124, v131, v129, vcc row_ror:8 row_mask:0xf bank_mask:0xf\n"
        "v_cndmask_b32_dpp v126, v135, v133, vcc row_ror:8 row_mask:0xf bank_mask:0xf\n"
        "s_mov_b64 vcc, s[56:57]\n"
        "s_nop 0\n"
        "v_cndmask_b32_dpp v121, v128, v130, vcc row_ror:8 row_mask:0xf bank_mask:0xf\n"
        "v_cndmask_b32_dpp v123, v132, v134, vcc row_ror:8 row_mask:0xf bank_mask:0xf\n"
        "v_cndmask_b32_dpp v125, v129, v131, vcc row_ror:8 row_mask:0xf bank_mask:0xf\n"
        "v_cndmask_b32_dpp v127, v133, v135, vcc row_ror:8 row_mask:0xf bank_mask:0xf\n"
        "s_waitcnt vmcnt(12)\n"
        "v_max3_u32 v24, v137, v139, v141\n"
        "v_max_u32 v24, v24, v143\n"
        "v_cmp_le_u32 vcc, s1, v24\n"
        "v_mfma_f32_16x16x32_bf16 v[104:107], v[120:123], v[120:123], 0\n"
        "ds_read_b64_tr_b16 a[236:237], v13 offset:4096\n"
        "s_cbranch_vccnz .LX21_RETRY_%=\n"
        ".LX21_DONE_%=:\n"
        "s_mov_b64 vcc, s[54:55]\n"
        "s_nop 0\n"
        "v_cndmask_b32_dpp v128, v138, v136, vcc row_ror:8 row_mask:0xf bank_mask:0xf\n"
        "v_mfma_f32_16x16x32_bf16 v[112:115], v[76:79], v[120:123], 0\n"
        "ds_read_b64_tr_b16 a[238:239], v42 offset:4096\n"
        "v_cndmask_b32_dpp v130, v142, v140, vcc row_ror:8 row_mask:0xf bank_mask:0xf\n"
        "v_mfma_f32_16x16x32_bf16 v[108:111], v[124:127], v[124:127], 0\n"
        "ds_read_b64_tr_b16 a[240:241], v14 offset:4096\n"
        "v_cndmask_b32_dpp v132, v139, v137, vcc row_ror:8 row_mask:0xf bank_mask:0xf\n"
        "v_cndmask_b32_dpp v134, v143, v141, vcc row_ror:8 row_mask:0xf bank_mask:0xf\n"
        "v_mfma_f32_16x16x32_bf16 v[116:119], v[76:79], v[124:127], 0\n"
        "s_waitcnt vmcnt(8) lgkmcnt(3)\n"
        "s_mov_b64 vcc, s[56:57]\n"
        "v_mfma_f32_16x16x32_bf16 v[48:51], a[224:227], v[120:123], 0\n"
        "ds_read_b64_tr_b16 a[242:243], v43 offset:4096\n"
        "v_cndmask_b32_dpp v129, v136, v138, vcc row_ror:8 row_mask:0xf bank_mask:0xf\n"
        "v_cndmask_b32_dpp v131, v140, v142, vcc row_ror:8 row_mask:0xf bank_mask:0xf\n"
        "v_mfma_f32_16x16x32_bf16 v[52:55], a[224:227], v[124:127], 0\n"
        "ds_read_b64_tr_b16 a[244:245], v19 offset:4096\n"
        "v_cndmask_b32_dpp v133, v137, v139, vcc row_ror:8 row_mask:0xf bank_mask:0xf\n"
        "v_mfma_f32_16x16x32_bf16 v[56:59], a[228:231], v[120:123], 0\n"
        "ds_read_b64_tr_b16 a[246:247], v44 offset:4096\n"
        "v_cndmask_b32_dpp v135, v141, v143, vcc row_ror:8 row_mask:0xf bank_mask:0xf\n"
        "v_max3_u32 v24, v145, v147, v149\n"
        "v_mfma_f32_16x16x32_bf16 v[60:63], a[228:231], v[124:127], 0\n"
        "v_max3_u32 v24, v24, v151, v153\n"
        "v_max3_u32 v24, v24, v155, v157\n"
        "v_mfma_f32_16x16x32_bf16 v[64:67], a[232:235], v[120:123], 0\n"
        "buffer_load_dwordx4 v[192:195], v0, s[36:39], s6 offen sc1\n"
        "v_max_u32 v24, v24, v159\n"
        "v_cmp_le_u32 vcc, s1, v24\n"
        "v_mfma_f32_16x16x32_bf16 v[68:71], a[232:235], v[124:127], 0\n"
        "buffer_load_dwordx4 v[196:199], v20, s[36:39], s6 offen sc1\n"
        "v_mfma_f32_16x16x32_bf16 v[104:107], v[128:131], v[128:131], v[104:107]\n"
        "ds_read_b64_tr_b16 a[248:249], v13 offset:8192\n"
        "s_cbranch_vccnz .LX22_RETRY_%=\n"
        ".LX22_DONE_%=:\n"
        "v_mfma_f32_16x16x32_bf16 v[112:115], v[76:79], v[128:131], v[112:115]\n"
        "ds_read_b64_tr_b16 a[250:251], v42 offset:8192\n"
        "s_mov_b64 vcc, s[54:55]\n"
        "v_mfma_f32_16x16x32_bf16 v[108:111], v[132:135], v[132:135], v[108:111]\n"
        "ds_read_b64_tr_b16 a[252:253], v14 offset:8192\n"
        "v_cndmask_b32_dpp v136, v146, v144, vcc row_ror:8 row_mask:0xf bank_mask:0xf\n"
        "v_mfma_f32_16x16x32_bf16 v[116:119], v[76:79], v[132:135], v[116:119]\n"
        "s_waitcnt lgkmcnt(3)\n"
        "v_cndmask_b32_dpp v138, v150, v148, vcc row_ror:8 row_mask:0xf bank_mask:0xf\n"
        "v_mfma_f32_16x16x32_bf16 v[48:51], a[236:239], v[128:131], v[48:51]\n"
        "ds_read_b64_tr_b16 a[254:255], v43 offset:8192\n"
        "v_cndmask_b32_dpp v140, v147, v145, vcc row_ror:8 row_mask:0xf bank_mask:0xf\n"
        "v_mfma_f32_16x16x32_bf16 v[52:55], a[236:239], v[132:135], v[52:55]\n"
        "ds_read_b64_tr_b16 a[224:225], v19 offset:8192\n"
        "v_cndmask_b32_dpp v142, v151, v149, vcc row_ror:8 row_mask:0xf bank_mask:0xf\n"
        "v_mfma_f32_16x16x32_bf16 v[56:59], a[240:243], v[128:131], v[56:59]\n"
        "ds_read_b64_tr_b16 a[226:227], v44 offset:8192\n"
        "s_mov_b64 vcc, s[56:57]\n"
        "v_mfma_f32_16x16x32_bf16 v[60:63], a[240:243], v[132:135], v[60:63]\n"
        "v_cndmask_b32_dpp v137, v144, v146, vcc row_ror:8 row_mask:0xf bank_mask:0xf\n"
        "v_cndmask_b32_dpp v139, v148, v150, vcc row_ror:8 row_mask:0xf bank_mask:0xf\n"
        "v_mfma_f32_16x16x32_bf16 v[64:67], a[244:247], v[128:131], v[64:67]\n"
        "buffer_load_dwordx4 v[200:203], v40, s[36:39], s6 offen sc1\n"
        "v_cndmask_b32_dpp v141, v145, v147, vcc row_ror:8 row_mask:0xf bank_mask:0xf\n"
        "v_mfma_f32_16x16x32_bf16 v[68:71], a[244:247], v[132:135], v[68:71]\n"
        "buffer_load_dwordx4 v[204:207], v41, s[36:39], s6 offen sc1\n"
        "v_cndmask_b32_dpp v143, v149, v151, vcc row_ror:8 row_mask:0xf bank_mask:0xf\n"
        "v_mfma_f32_16x16x32_bf16 v[104:107], v[136:139], v[136:139], v[104:107]\n"
        "ds_read_b64_tr_b16 a[228:229], v13 offset:12288\n"
        "v_cndmask_b32_dpp v145, v152, v154, vcc row_ror:8 row_mask:0xf bank_mask:0xf\n"
        "v_cndmask_b32_dpp v147, v156, v158, vcc row_ror:8 row_mask:0xf bank_mask:0xf\n"
        "v_mfma_f32_16x16x32_bf16 v[112:115], v[76:79], v[136:139], v[112:115]\n"
        "ds_read_b64_tr_b16 a[230:231], v42 offset:12288\n"
        "v_cndmask_b32_dpp v149, v153, v155, vcc row_ror:8 row_mask:0xf bank_mask:0xf\n"
        "v_mfma_f32_16x16x32_bf16 v[108:111], v[140:143], v[140:143], v[108:111]\n"
        "ds_read_b64_tr_b16 a[232:233], v14 offset:12288\n"
        "s_cbranch_execz .LWARM_RET_%=\n"
        "v_cndmask_b32_dpp v151, v157, v159, vcc row_ror:8 row_mask:0xf bank_mask:0xf\n"
        "s_mov_b64 vcc, s[54:55]\n"
        "v_mfma_f32_16x16x32_bf16 v[116:119], v[76:79], v[140:143], v[116:119]\n"
        "s_waitcnt vmcnt(8) lgkmcnt(3)\n"
        "v_cndmask_b32_dpp v144, v154, v152, vcc row_ror:8 row_mask:0xf bank_mask:0xf\n"
        "v_mfma_f32_16x16x32_bf16 v[48:51], a[248:251], v[136:139], v[48:51]\n"
        "ds_read_b64_tr_b16 a[234:235], v43 offset:12288\n"
        "v_cndmask_b32_dpp v146, v158, v156, vcc row_ror:8 row_mask:0xf bank_mask:0xf\n"
        "v_cndmask_b32_dpp v148, v155, v153, vcc row_ror:8 row_mask:0xf bank_mask:0xf\n"
        "v_mfma_f32_16x16x32_bf16 v[52:55], a[248:251], v[140:143], v[52:55]\n"
        "ds_read_b64_tr_b16 a[236:237], v19 offset:12288\n"
        "v_cndmask_b32_dpp v150, v159, v157, vcc row_ror:8 row_mask:0xf bank_mask:0xf\n"
        "v_mfma_f32_16x16x32_bf16 v[56:59], a[252:255], v[136:139], v[56:59]\n"
        "ds_read_b64_tr_b16 a[238:239], v44 offset:12288\n"
        "v_max3_u32 v24, v161, v163, v165\n"
        "v_max3_u32 v24, v24, v167, v169\n"
        "v_mfma_f32_16x16x32_bf16 v[60:63], a[252:255], v[140:143], v[60:63]\n"
        "v_max3_u32 v24, v24, v171, v173\n"
        "v_max_u32 v24, v24, v175\n"
        "v_mfma_f32_16x16x32_bf16 v[64:67], a[224:227], v[136:139], v[64:67]\n"
        "buffer_load_dwordx4 v[208:211], v0, s[36:39], s23 offen sc1\n"
        "v_cmp_le_u32 vcc, s1, v24\n"
        "v_mfma_f32_16x16x32_bf16 v[68:71], a[224:227], v[140:143], v[68:71]\n"
        "buffer_load_dwordx4 v[212:215], v20, s[36:39], s23 offen sc1\n"
        "v_mfma_f32_16x16x32_bf16 v[104:107], v[144:147], v[144:147], v[104:107]\n"
        "ds_read_b64_tr_b16 a[240:241], v13 offset:16384\n"
        "s_cbranch_vccnz .LX24_RETRY_%=\n"
        ".LX24_DONE_%=:\n"
        "v_mfma_f32_16x16x32_bf16 v[112:115], v[76:79], v[144:147], v[112:115]\n"
        "ds_read_b64_tr_b16 a[242:243], v42 offset:16384\n"
        "s_mov_b64 vcc, s[54:55]\n"
        "v_mfma_f32_16x16x32_bf16 v[108:111], v[148:151], v[148:151], v[108:111]\n"
        "ds_read_b64_tr_b16 a[244:245], v14 offset:16384\n"
        "v_cndmask_b32_dpp v152, v162, v160, vcc row_ror:8 row_mask:0xf bank_mask:0xf\n"
        "v_mfma_f32_16x16x32_bf16 v[116:119], v[76:79], v[148:151], v[116:119]\n"
        "s_waitcnt lgkmcnt(3)\n"
        "v_cndmask_b32_dpp v154, v166, v164, vcc row_ror:8 row_mask:0xf bank_mask:0xf\n"
        "v_mfma_f32_16x16x32_bf16 v[48:51], a[228:231], v[144:147], v[48:51]\n"
        "ds_read_b64_tr_b16 a[246:247], v43 offset:16384\n"
        "v_cndmask_b32_dpp v156, v163, v161, vcc row_ror:8 row_mask:0xf bank_mask:0xf\n"
        "v_mfma_f32_16x16x32_bf16 v[52:55], a[228:231], v[148:151], v[52:55]\n"
        "ds_read_b64_tr_b16 a[248:249], v19 offset:16384\n"
        "v_cndmask_b32_dpp v158, v167, v165, vcc row_ror:8 row_mask:0xf bank_mask:0xf\n"
        "v_mfma_f32_16x16x32_bf16 v[56:59], a[232:235], v[144:147], v[56:59]\n"
        "ds_read_b64_tr_b16 a[250:251], v44 offset:16384\n"
        "s_mov_b64 vcc, s[56:57]\n"
        "v_mfma_f32_16x16x32_bf16 v[60:63], a[232:235], v[148:151], v[60:63]\n"
        "v_cndmask_b32_dpp v153, v160, v162, vcc row_ror:8 row_mask:0xf bank_mask:0xf\n"
        "v_cndmask_b32_dpp v155, v164, v166, vcc row_ror:8 row_mask:0xf bank_mask:0xf\n"
        "v_mfma_f32_16x16x32_bf16 v[64:67], a[236:239], v[144:147], v[64:67]\n"
        "buffer_load_dwordx4 v[216:219], v40, s[36:39], s23 offen sc1\n"
        "v_cndmask_b32_dpp v157, v161, v163, vcc row_ror:8 row_mask:0xf bank_mask:0xf\n"
        "v_mfma_f32_16x16x32_bf16 v[68:71], a[236:239], v[148:151], v[68:71]\n"
        "buffer_load_dwordx4 v[220:223], v41, s[36:39], s23 offen sc1\n"
        "v_cndmask_b32_dpp v159, v165, v167, vcc row_ror:8 row_mask:0xf bank_mask:0xf\n"
        "v_mfma_f32_16x16x32_bf16 v[104:107], v[152:155], v[152:155], v[104:107]\n"
        "ds_read_b64_tr_b16 a[252:253], v13 offset:20480\n"
        "v_cndmask_b32_dpp v161, v168, v170, vcc row_ror:8 row_mask:0xf bank_mask:0xf\n"
        "v_cndmask_b32_dpp v163, v172, v174, vcc row_ror:8 row_mask:0xf bank_mask:0xf\n"
        "v_mfma_f32_16x16x32_bf16 v[112:115], v[76:79], v[152:155], v[112:115]\n"
        "ds_read_b64_tr_b16 a[254:255], v42 offset:20480\n"
        "v_cndmask_b32_dpp v165, v169, v171, vcc row_ror:8 row_mask:0xf bank_mask:0xf\n"
        "v_mfma_f32_16x16x32_bf16 v[108:111], v[156:159], v[156:159], v[108:111]\n"
        "ds_read_b64_tr_b16 a[224:225], v14 offset:20480\n"
        "v_cndmask_b32_dpp v167, v173, v175, vcc row_ror:8 row_mask:0xf bank_mask:0xf\n"
        "s_mov_b64 vcc, s[54:55]\n"
        "v_mfma_f32_16x16x32_bf16 v[116:119], v[76:79], v[156:159], v[116:119]\n"
        "s_waitcnt vmcnt(8) lgkmcnt(3)\n"
        "v_cndmask_b32_dpp v160, v170, v168, vcc row_ror:8 row_mask:0xf bank_mask:0xf\n"
        "v_mfma_f32_16x16x32_bf16 v[48:51], a[240:243], v[152:155], v[48:51]\n"
        "ds_read_b64_tr_b16 a[226:227], v43 offset:20480\n"
        "v_cndmask_b32_dpp v162, v174, v172, vcc row_ror:8 row_mask:0xf bank_mask:0xf\n"
        "v_cndmask_b32_dpp v164, v171, v169, vcc row_ror:8 row_mask:0xf bank_mask:0xf\n"
        "v_mfma_f32_16x16x32_bf16 v[52:55], a[240:243], v[156:159], v[52:55]\n"
        "ds_read_b64_tr_b16 a[228:229], v19 offset:20480\n"
        "v_cndmask_b32_dpp v166, v175, v173, vcc row_ror:8 row_mask:0xf bank_mask:0xf\n"
        "v_mfma_f32_16x16x32_bf16 v[56:59], a[244:247], v[152:155], v[56:59]\n"
        "ds_read_b64_tr_b16 a[230:231], v44 offset:20480\n"
        "v_max3_u32 v24, v177, v179, v181\n"
        "v_max3_u32 v24, v24, v183, v185\n"
        "v_mfma_f32_16x16x32_bf16 v[60:63], a[244:247], v[156:159], v[60:63]\n"
        "v_max3_u32 v24, v24, v187, v189\n"
        "v_max_u32 v24, v24, v191\n"
        "v_mfma_f32_16x16x32_bf16 v[64:67], a[248:251], v[152:155], v[64:67]\n"
        "buffer_load_dwordx4 v[224:227], v0, s[36:39], s22 offen sc1\n"
        "v_cmp_le_u32 vcc, s1, v24\n"
        "v_mfma_f32_16x16x32_bf16 v[68:71], a[248:251], v[156:159], v[68:71]\n"
        "buffer_load_dwordx4 v[228:231], v20, s[36:39], s22 offen sc1\n"
        "v_mfma_f32_16x16x32_bf16 v[104:107], v[160:163], v[160:163], v[104:107]\n"
        "ds_read_b64_tr_b16 a[232:233], v13 offset:24576\n"
        "s_cbranch_vccnz .LX26_RETRY_%=\n"
        ".LX26_DONE_%=:\n"
        "v_mfma_f32_16x16x32_bf16 v[112:115], v[76:79], v[160:163], v[112:115]\n"
        "ds_read_b64_tr_b16 a[234:235], v42 offset:24576\n"
        "s_mov_b64 vcc, s[54:55]\n"
        "v_mfma_f32_16x16x32_bf16 v[108:111], v[164:167], v[164:167], v[108:111]\n"
        "ds_read_b64_tr_b16 a[236:237], v14 offset:24576\n"
        "v_cndmask_b32_dpp v168, v178, v176, vcc row_ror:8 row_mask:0xf bank_mask:0xf\n"
        "v_mfma_f32_16x16x32_bf16 v[116:119], v[76:79], v[164:167], v[116:119]\n"
        "s_waitcnt lgkmcnt(3)\n"
        "v_cndmask_b32_dpp v170, v182, v180, vcc row_ror:8 row_mask:0xf bank_mask:0xf\n"
        "v_mfma_f32_16x16x32_bf16 v[48:51], a[252:255], v[160:163], v[48:51]\n"
        "ds_read_b64_tr_b16 a[238:239], v43 offset:24576\n"
        "v_cndmask_b32_dpp v172, v179, v177, vcc row_ror:8 row_mask:0xf bank_mask:0xf\n"
        "v_mfma_f32_16x16x32_bf16 v[52:55], a[252:255], v[164:167], v[52:55]\n"
        "ds_read_b64_tr_b16 a[240:241], v19 offset:24576\n"
        "v_cndmask_b32_dpp v174, v183, v181, vcc row_ror:8 row_mask:0xf bank_mask:0xf\n"
        "v_mfma_f32_16x16x32_bf16 v[56:59], a[224:227], v[160:163], v[56:59]\n"
        "ds_read_b64_tr_b16 a[242:243], v44 offset:24576\n"
        "s_mov_b64 vcc, s[56:57]\n"
        "v_mfma_f32_16x16x32_bf16 v[60:63], a[224:227], v[164:167], v[60:63]\n"
        "v_cndmask_b32_dpp v169, v176, v178, vcc row_ror:8 row_mask:0xf bank_mask:0xf\n"
        "v_cndmask_b32_dpp v171, v180, v182, vcc row_ror:8 row_mask:0xf bank_mask:0xf\n"
        "v_mfma_f32_16x16x32_bf16 v[64:67], a[228:231], v[160:163], v[64:67]\n"
        "buffer_load_dwordx4 v[232:235], v40, s[36:39], s22 offen sc1\n"
        "v_cndmask_b32_dpp v173, v177, v179, vcc row_ror:8 row_mask:0xf bank_mask:0xf\n"
        "v_mfma_f32_16x16x32_bf16 v[68:71], a[228:231], v[164:167], v[68:71]\n"
        "buffer_load_dwordx4 v[236:239], v41, s[36:39], s22 offen sc1\n"
        "v_cndmask_b32_dpp v175, v181, v183, vcc row_ror:8 row_mask:0xf bank_mask:0xf\n"
        "v_mfma_f32_16x16x32_bf16 v[104:107], v[168:171], v[168:171], v[104:107]\n"
        "ds_read_b64_tr_b16 a[244:245], v13 offset:28672\n"
        "v_cndmask_b32_dpp v177, v184, v186, vcc row_ror:8 row_mask:0xf bank_mask:0xf\n"
        "v_cndmask_b32_dpp v179, v188, v190, vcc row_ror:8 row_mask:0xf bank_mask:0xf\n"
        "v_mfma_f32_16x16x32_bf16 v[112:115], v[76:79], v[168:171], v[112:115]\n"
        "ds_read_b64_tr_b16 a[246:247], v42 offset:28672\n"
        "v_cndmask_b32_dpp v181, v185, v187, vcc row_ror:8 row_mask:0xf bank_mask:0xf\n"
        "v_mfma_f32_16x16x32_bf16 v[108:111], v[172:175], v[172:175], v[108:111]\n"
        "ds_read_b64_tr_b16 a[248:249], v14 offset:28672\n"
        "v_cndmask_b32_dpp v183, v189, v191, vcc row_ror:8 row_mask:0xf bank_mask:0xf\n"
        "s_mov_b64 vcc, s[54:55]\n"
        "v_mfma_f32_16x16x32_bf16 v[116:119], v[76:79], v[172:175], v[116:119]\n"
        "s_waitcnt vmcnt(8) lgkmcnt(3)\n"
        "v_cndmask_b32_dpp v176, v186, v184, vcc row_ror:8 row_mask:0xf bank_mask:0xf\n"
        "v_mfma_f32_16x16x32_bf16 v[48:51], a[232:235], v[168:171], v[48:51]\n"
        "ds_read_b64_tr_b16 a[250:251], v43 offset:28672\n"
        "v_cndmask_b32_dpp v178, v190, v188, vcc row_ror:8 row_mask:0xf bank_mask:0xf\n"
        "v_cndmask_b32_dpp v180, v187, v185, vcc row_ror:8 row_mask:0xf bank_mask:0xf\n"
        "v_mfma_f32_16x16x32_bf16 v[52:55], a[232:235], v[172:175], v[52:55]\n"
        "ds_read_b64_tr_b16 a[252:253], v19 offset:28672\n"
        "v_cndmask_b32_dpp v182, v191, v189, vcc row_ror:8 row_mask:0xf bank_mask:0xf\n"
        "v_mfma_f32_16x16x32_bf16 v[56:59], a[236:239], v[168:171], v[56:59]\n"
        "ds_read_b64_tr_b16 a[254:255], v44 offset:28672\n"
        "v_max3_u32 v24, v193, v195, v197\n"
        "v_max3_u32 v24, v24, v199, v201\n"
        "v_mfma_f32_16x16x32_bf16 v[60:63], a[236:239], v[172:175], v[60:63]\n"
        "v_max3_u32 v24, v24, v203, v205\n"
        "v_max_u32 v24, v24, v207\n"
        "v_mfma_f32_16x16x32_bf16 v[64:67], a[240:243], v[168:171], v[64:67]\n"
        "buffer_load_dwordx4 v[240:243], v0, s[36:39], s18 offen sc1\n"
        "v_cmp_le_u32 vcc, s1, v24\n"
        "v_mfma_f32_16x16x32_bf16 v[68:71], a[240:243], v[172:175], v[68:71]\n"
        "buffer_load_dwordx4 v[244:247], v20, s[36:39], s18 offen sc1\n"
        "v_mfma_f32_16x16x32_bf16 v[104:107], v[176:179], v[176:179], v[104:107]\n"
        "ds_read_b64_tr_b16 a[224:225], v13 offset:32768\n"
        "s_cbranch_vccnz .LX28_RETRY_%=\n"
        ".LX28_DONE_%=:\n"
        "v_mfma_f32_16x16x32_bf16 v[112:115], v[76:79], v[176:179], v[112:115]\n"
        "ds_read_b64_tr_b16 a[226:227], v42 offset:32768\n"
        "s_mov_b64 vcc, s[54:55]\n"
        "v_mfma_f32_16x16x32_bf16 v[108:111], v[180:183], v[180:183], v[108:111]\n"
        "ds_read_b64_tr_b16 a[228:229], v14 offset:32768\n"
        "v_cndmask_b32_dpp v184, v194, v192, vcc row_ror:8 row_mask:0xf bank_mask:0xf\n"
        "v_mfma_f32_16x16x32_bf16 v[116:119], v[76:79], v[180:183], v[116:119]\n"
        "s_waitcnt lgkmcnt(3)\n"
        "v_cndmask_b32_dpp v186, v198, v196, vcc row_ror:8 row_mask:0xf bank_mask:0xf\n"
        "v_mfma_f32_16x16x32_bf16 v[48:51], a[244:247], v[176:179], v[48:51]\n"
        "ds_read_b64_tr_b16 a[230:231], v43 offset:32768\n"
        "v_cndmask_b32_dpp v188, v195, v193, vcc row_ror:8 row_mask:0xf bank_mask:0xf\n"
        "v_mfma_f32_16x16x32_bf16 v[52:55], a[244:247], v[180:183], v[52:55]\n"
        "ds_read_b64_tr_b16 a[232:233], v19 offset:32768\n"
        "v_cndmask_b32_dpp v190, v199, v197, vcc row_ror:8 row_mask:0xf bank_mask:0xf\n"
        "v_mfma_f32_16x16x32_bf16 v[56:59], a[248:251], v[176:179], v[56:59]\n"
        "ds_read_b64_tr_b16 a[234:235], v44 offset:32768\n"
        "s_mov_b64 vcc, s[56:57]\n"
        "v_mfma_f32_16x16x32_bf16 v[60:63], a[248:251], v[180:183], v[60:63]\n"
        "v_cndmask_b32_dpp v185, v192, v194, vcc row_ror:8 row_mask:0xf bank_mask:0xf\n"
        "v_cndmask_b32_dpp v187, v196, v198, vcc row_ror:8 row_mask:0xf bank_mask:0xf\n"
        "v_mfma_f32_16x16x32_bf16 v[64:67], a[252:255], v[176:179], v[64:67]\n"
        "buffer_load_dwordx4 v[248:251], v40, s[36:39], s18 offen sc1\n"
        "v_cndmask_b32_dpp v189, v193, v195, vcc row_ror:8 row_mask:0xf bank_mask:0xf\n"
        "v_mfma_f32_16x16x32_bf16 v[68:71], a[252:255], v[180:183], v[68:71]\n"
        "buffer_load_dwordx4 v[252:255], v41, s[36:39], s18 offen sc1\n"
        "v_cndmask_b32_dpp v191, v197, v199, vcc row_ror:8 row_mask:0xf bank_mask:0xf\n"
        "v_mfma_f32_16x16x32_bf16 v[104:107], v[184:187], v[184:187], v[104:107]\n"
        "ds_read_b64_tr_b16 a[236:237], v13 offset:36864\n"
        "v_cndmask_b32_dpp v193, v200, v202, vcc row_ror:8 row_mask:0xf bank_mask:0xf\n"
        "v_cndmask_b32_dpp v195, v204, v206, vcc row_ror:8 row_mask:0xf bank_mask:0xf\n"
        "v_mfma_f32_16x16x32_bf16 v[112:115], v[76:79], v[184:187], v[112:115]\n"
        "ds_read_b64_tr_b16 a[238:239], v42 offset:36864\n"
        "v_cndmask_b32_dpp v197, v201, v203, vcc row_ror:8 row_mask:0xf bank_mask:0xf\n"
        "v_mfma_f32_16x16x32_bf16 v[108:111], v[188:191], v[188:191], v[108:111]\n"
        "ds_read_b64_tr_b16 a[240:241], v14 offset:36864\n"
        "v_cndmask_b32_dpp v199, v205, v207, vcc row_ror:8 row_mask:0xf bank_mask:0xf\n"
        "s_mov_b64 vcc, s[54:55]\n"
        "v_mfma_f32_16x16x32_bf16 v[116:119], v[76:79], v[188:191], v[116:119]\n"
        "s_waitcnt vmcnt(8) lgkmcnt(3)\n"
        "v_cndmask_b32_dpp v192, v202, v200, vcc row_ror:8 row_mask:0xf bank_mask:0xf\n"
        "v_mfma_f32_16x16x32_bf16 v[48:51], a[224:227], v[184:187], v[48:51]\n"
        "ds_read_b64_tr_b16 a[242:243], v43 offset:36864\n"
        "v_cndmask_b32_dpp v194, v206, v204, vcc row_ror:8 row_mask:0xf bank_mask:0xf\n"
        "v_cndmask_b32_dpp v196, v203, v201, vcc row_ror:8 row_mask:0xf bank_mask:0xf\n"
        "v_mfma_f32_16x16x32_bf16 v[52:55], a[224:227], v[188:191], v[52:55]\n"
        "ds_read_b64_tr_b16 a[244:245], v19 offset:36864\n"
        "v_cndmask_b32_dpp v198, v207, v205, vcc row_ror:8 row_mask:0xf bank_mask:0xf\n"
        "v_mfma_f32_16x16x32_bf16 v[56:59], a[228:231], v[184:187], v[56:59]\n"
        "ds_read_b64_tr_b16 a[246:247], v44 offset:36864\n"
        "v_max3_u32 v24, v209, v211, v213\n"
        "v_max3_u32 v24, v24, v215, v217\n"
        "v_mfma_f32_16x16x32_bf16 v[60:63], a[228:231], v[188:191], v[60:63]\n"
        "v_max3_u32 v24, v24, v219, v221\n"
        "v_max_u32 v24, v24, v223\n"
        "v_mfma_f32_16x16x32_bf16 v[64:67], a[232:235], v[184:187], v[64:67]\n"
        "buffer_load_dwordx4 a[52:55], v4, s[36:39], s95 offen offset:1024 sc1\n"
        "v_cmp_le_u32 vcc, s1, v24\n"
        "v_mfma_f32_16x16x32_bf16 v[68:71], a[232:235], v[188:191], v[68:71]\n"
        "buffer_load_dwordx4 a[92:95], v4, s[36:39], s27 offen offset:3072 sc1\n"
        "v_mfma_f32_16x16x32_bf16 v[104:107], v[192:195], v[192:195], v[104:107]\n"
        "ds_read_b64_tr_b16 a[248:249], v13 offset:40960\n"
        "s_cbranch_vccnz .LX210_RETRY_%=\n"
        ".LX210_DONE_%=:\n"
        "v_mfma_f32_16x16x32_bf16 v[112:115], v[76:79], v[192:195], v[112:115]\n"
        "ds_read_b64_tr_b16 a[250:251], v42 offset:40960\n"
        "s_mov_b64 vcc, s[54:55]\n"
        "v_mfma_f32_16x16x32_bf16 v[108:111], v[196:199], v[196:199], v[108:111]\n"
        "ds_read_b64_tr_b16 a[252:253], v14 offset:40960\n"
        "v_cndmask_b32_dpp v200, v210, v208, vcc row_ror:8 row_mask:0xf bank_mask:0xf\n"
        "v_mfma_f32_16x16x32_bf16 v[116:119], v[76:79], v[196:199], v[116:119]\n"
        "s_waitcnt lgkmcnt(3)\n"
        "v_cndmask_b32_dpp v202, v214, v212, vcc row_ror:8 row_mask:0xf bank_mask:0xf\n"
        "v_mfma_f32_16x16x32_bf16 v[48:51], a[236:239], v[192:195], v[48:51]\n"
        "ds_read_b64_tr_b16 a[254:255], v43 offset:40960\n"
        "v_cndmask_b32_dpp v204, v211, v209, vcc row_ror:8 row_mask:0xf bank_mask:0xf\n"
        "v_mfma_f32_16x16x32_bf16 v[52:55], a[236:239], v[196:199], v[52:55]\n"
        "ds_read_b64_tr_b16 a[224:225], v19 offset:40960\n"
        "v_cndmask_b32_dpp v206, v215, v213, vcc row_ror:8 row_mask:0xf bank_mask:0xf\n"
        "v_mfma_f32_16x16x32_bf16 v[56:59], a[240:243], v[192:195], v[56:59]\n"
        "ds_read_b64_tr_b16 a[226:227], v44 offset:40960\n"
        "s_mov_b64 vcc, s[56:57]\n"
        "v_mfma_f32_16x16x32_bf16 v[60:63], a[240:243], v[196:199], v[60:63]\n"
        "v_cndmask_b32_dpp v201, v208, v210, vcc row_ror:8 row_mask:0xf bank_mask:0xf\n"
        "v_cndmask_b32_dpp v203, v212, v214, vcc row_ror:8 row_mask:0xf bank_mask:0xf\n"
        "v_mfma_f32_16x16x32_bf16 v[64:67], a[244:247], v[192:195], v[64:67]\n"
        "buffer_load_dwordx4 a[56:59], v4, s[36:39], s95 offen offset:2048 sc1\n"
        "v_cndmask_b32_dpp v205, v209, v211, vcc row_ror:8 row_mask:0xf bank_mask:0xf\n"
        "v_mfma_f32_16x16x32_bf16 v[68:71], a[244:247], v[196:199], v[68:71]\n"
        "buffer_load_dwordx4 a[60:63], v4, s[36:39], s95 offen offset:3072 sc1\n"
        "v_cndmask_b32_dpp v207, v213, v215, vcc row_ror:8 row_mask:0xf bank_mask:0xf\n"
        "v_mfma_f32_16x16x32_bf16 v[104:107], v[200:203], v[200:203], v[104:107]\n"
        "ds_read_b64_tr_b16 a[228:229], v13 offset:45056\n"
        "v_cndmask_b32_dpp v209, v216, v218, vcc row_ror:8 row_mask:0xf bank_mask:0xf\n"
        "v_cndmask_b32_dpp v211, v220, v222, vcc row_ror:8 row_mask:0xf bank_mask:0xf\n"
        "v_mfma_f32_16x16x32_bf16 v[112:115], v[76:79], v[200:203], v[112:115]\n"
        "ds_read_b64_tr_b16 a[230:231], v42 offset:45056\n"
        "v_cndmask_b32_dpp v213, v217, v219, vcc row_ror:8 row_mask:0xf bank_mask:0xf\n"
        "v_mfma_f32_16x16x32_bf16 v[108:111], v[204:207], v[204:207], v[108:111]\n"
        "ds_read_b64_tr_b16 a[232:233], v14 offset:45056\n"
        "v_cndmask_b32_dpp v215, v221, v223, vcc row_ror:8 row_mask:0xf bank_mask:0xf\n"
        "s_mov_b64 vcc, s[54:55]\n"
        "v_mfma_f32_16x16x32_bf16 v[116:119], v[76:79], v[204:207], v[116:119]\n"
        "s_waitcnt vmcnt(8) lgkmcnt(3)\n"
        "v_cndmask_b32_dpp v208, v218, v216, vcc row_ror:8 row_mask:0xf bank_mask:0xf\n"
        "v_mfma_f32_16x16x32_bf16 v[48:51], a[248:251], v[200:203], v[48:51]\n"
        "ds_read_b64_tr_b16 a[234:235], v43 offset:45056\n"
        "v_cndmask_b32_dpp v210, v222, v220, vcc row_ror:8 row_mask:0xf bank_mask:0xf\n"
        "v_cndmask_b32_dpp v212, v219, v217, vcc row_ror:8 row_mask:0xf bank_mask:0xf\n"
        "v_mfma_f32_16x16x32_bf16 v[52:55], a[248:251], v[204:207], v[52:55]\n"
        "ds_read_b64_tr_b16 a[236:237], v19 offset:45056\n"
        "v_cndmask_b32_dpp v214, v223, v221, vcc row_ror:8 row_mask:0xf bank_mask:0xf\n"
        "v_mfma_f32_16x16x32_bf16 v[56:59], a[252:255], v[200:203], v[56:59]\n"
        "ds_read_b64_tr_b16 a[238:239], v44 offset:45056\n"
        "v_max3_u32 v24, v225, v227, v229\n"
        "v_max3_u32 v24, v24, v231, v233\n"
        "v_mfma_f32_16x16x32_bf16 v[60:63], a[252:255], v[204:207], v[60:63]\n"
        "v_max3_u32 v24, v24, v235, v237\n"
        "v_max_u32 v24, v24, v239\n"
        "v_mfma_f32_16x16x32_bf16 v[64:67], a[224:227], v[200:203], v[64:67]\n"
        "buffer_load_dwordx4 a[96:99], v102, s[36:39], s92 offen sc1\n"
        "v_cmp_le_u32 vcc, s1, v24\n"
        "v_mfma_f32_16x16x32_bf16 v[68:71], a[224:227], v[204:207], v[68:71]\n"
        "buffer_load_dwordx4 a[100:103], v102, s[36:39], s92 offen offset:1024 sc1\n"
        "v_mfma_f32_16x16x32_bf16 v[104:107], v[208:211], v[208:211], v[104:107]\n"
        "ds_read_b64_tr_b16 a[240:241], v13 offset:49152\n"
        "s_cbranch_vccnz .LX212_RETRY_%=\n"
        ".LX212_DONE_%=:\n"
        "v_mfma_f32_16x16x32_bf16 v[112:115], v[76:79], v[208:211], v[112:115]\n"
        "ds_read_b64_tr_b16 a[242:243], v42 offset:49152\n"
        "s_mov_b64 vcc, s[54:55]\n"
        "v_mfma_f32_16x16x32_bf16 v[108:111], v[212:215], v[212:215], v[108:111]\n"
        "ds_read_b64_tr_b16 a[244:245], v14 offset:49152\n"
        "v_cndmask_b32_dpp v216, v226, v224, vcc row_ror:8 row_mask:0xf bank_mask:0xf\n"
        "v_mfma_f32_16x16x32_bf16 v[116:119], v[76:79], v[212:215], v[116:119]\n"
        "s_waitcnt lgkmcnt(3)\n"
        "v_cndmask_b32_dpp v218, v230, v228, vcc row_ror:8 row_mask:0xf bank_mask:0xf\n"
        "v_mfma_f32_16x16x32_bf16 v[48:51], a[228:231], v[208:211], v[48:51]\n"
        "ds_read_b64_tr_b16 a[246:247], v43 offset:49152\n"
        "v_cndmask_b32_dpp v220, v227, v225, vcc row_ror:8 row_mask:0xf bank_mask:0xf\n"
        "v_mfma_f32_16x16x32_bf16 v[52:55], a[228:231], v[212:215], v[52:55]\n"
        "ds_read_b64_tr_b16 a[248:249], v19 offset:49152\n"
        "v_cndmask_b32_dpp v222, v231, v229, vcc row_ror:8 row_mask:0xf bank_mask:0xf\n"
        "v_mfma_f32_16x16x32_bf16 v[56:59], a[232:235], v[208:211], v[56:59]\n"
        "ds_read_b64_tr_b16 a[250:251], v44 offset:49152\n"
        "s_mov_b64 vcc, s[56:57]\n"
        "v_mfma_f32_16x16x32_bf16 v[60:63], a[232:235], v[212:215], v[60:63]\n"
        "v_cndmask_b32_dpp v217, v224, v226, vcc row_ror:8 row_mask:0xf bank_mask:0xf\n"
        "v_cndmask_b32_dpp v219, v228, v230, vcc row_ror:8 row_mask:0xf bank_mask:0xf\n"
        "v_mfma_f32_16x16x32_bf16 v[64:67], a[236:239], v[208:211], v[64:67]\n"
        "buffer_load_dwordx4 a[104:107], v102, s[36:39], s92 offen offset:2048 sc1\n"
        "v_cndmask_b32_dpp v221, v225, v227, vcc row_ror:8 row_mask:0xf bank_mask:0xf\n"
        "v_mfma_f32_16x16x32_bf16 v[68:71], a[236:239], v[212:215], v[68:71]\n"
        "buffer_load_dwordx4 a[108:111], v102, s[36:39], s92 offen offset:3072 sc1\n"
        "v_cndmask_b32_dpp v223, v229, v231, vcc row_ror:8 row_mask:0xf bank_mask:0xf\n"
        "v_mfma_f32_16x16x32_bf16 v[104:107], v[216:219], v[216:219], v[104:107]\n"
        "ds_read_b64_tr_b16 a[252:253], v13 offset:53248\n"
        "v_cndmask_b32_dpp v225, v232, v234, vcc row_ror:8 row_mask:0xf bank_mask:0xf\n"
        "v_cndmask_b32_dpp v227, v236, v238, vcc row_ror:8 row_mask:0xf bank_mask:0xf\n"
        "v_mfma_f32_16x16x32_bf16 v[112:115], v[76:79], v[216:219], v[112:115]\n"
        "ds_read_b64_tr_b16 a[254:255], v42 offset:53248\n"
        "v_cndmask_b32_dpp v229, v233, v235, vcc row_ror:8 row_mask:0xf bank_mask:0xf\n"
        "v_mfma_f32_16x16x32_bf16 v[108:111], v[220:223], v[220:223], v[108:111]\n"
        "ds_read_b64_tr_b16 a[224:225], v14 offset:53248\n"
        "v_cndmask_b32_dpp v231, v237, v239, vcc row_ror:8 row_mask:0xf bank_mask:0xf\n"
        "s_mov_b64 vcc, s[54:55]\n"
        "v_mfma_f32_16x16x32_bf16 v[116:119], v[76:79], v[220:223], v[116:119]\n"
        "s_waitcnt vmcnt(8) lgkmcnt(3)\n"
        "v_cndmask_b32_dpp v224, v234, v232, vcc row_ror:8 row_mask:0xf bank_mask:0xf\n"
        "v_mfma_f32_16x16x32_bf16 v[48:51], a[240:243], v[216:219], v[48:51]\n"
        "ds_read_b64_tr_b16 a[226:227], v43 offset:53248\n"
        "v_cndmask_b32_dpp v226, v238, v236, vcc row_ror:8 row_mask:0xf bank_mask:0xf\n"
        "v_cndmask_b32_dpp v228, v235, v233, vcc row_ror:8 row_mask:0xf bank_mask:0xf\n"
        "v_mfma_f32_16x16x32_bf16 v[52:55], a[240:243], v[220:223], v[52:55]\n"
        "ds_read_b64_tr_b16 a[228:229], v19 offset:53248\n"
        "v_cndmask_b32_dpp v230, v239, v237, vcc row_ror:8 row_mask:0xf bank_mask:0xf\n"
        "v_mfma_f32_16x16x32_bf16 v[56:59], a[244:247], v[216:219], v[56:59]\n"
        "ds_read_b64_tr_b16 a[230:231], v44 offset:53248\n"
        "v_max3_u32 v24, v241, v243, v245\n"
        "v_max3_u32 v24, v24, v247, v249\n"
        "v_mfma_f32_16x16x32_bf16 v[60:63], a[244:247], v[220:223], v[60:63]\n"
        "v_max3_u32 v24, v24, v251, v253\n"
        "v_max_u32 v24, v24, v255\n"
        "v_mfma_f32_16x16x32_bf16 v[64:67], a[248:251], v[216:219], v[64:67]\n"
        "buffer_load_dwordx4 a[160:163], v103, s[36:39], s92 offen sc1\n"
        "v_cmp_le_u32 vcc, s1, v24\n"
        "v_mfma_f32_16x16x32_bf16 v[68:71], a[248:251], v[220:223], v[68:71]\n"
        "buffer_load_dwordx4 a[112:115], v102, s[36:39], s93 offen sc1\n"
        "v_mfma_f32_16x16x32_bf16 v[104:107], v[224:227], v[224:227], v[104:107]\n"
        "ds_read_b64_tr_b16 a[232:233], v13 offset:57344\n"
        "s_cbranch_vccnz .LX214_RETRY_%=\n"
        ".LX214_DONE_%=:\n"
        "v_mfma_f32_16x16x32_bf16 v[112:115], v[76:79], v[224:227], v[112:115]\n"
        "ds_read_b64_tr_b16 a[234:235], v42 offset:57344\n"
        "s_mov_b64 vcc, s[54:55]\n"
        "v_mfma_f32_16x16x32_bf16 v[108:111], v[228:231], v[228:231], v[108:111]\n"
        "ds_read_b64_tr_b16 a[236:237], v14 offset:57344\n"
        "v_cndmask_b32_dpp v232, v242, v240, vcc row_ror:8 row_mask:0xf bank_mask:0xf\n"
        "v_mfma_f32_16x16x32_bf16 v[116:119], v[76:79], v[228:231], v[116:119]\n"
        "s_waitcnt lgkmcnt(3)\n"
        "v_cndmask_b32_dpp v234, v246, v244, vcc row_ror:8 row_mask:0xf bank_mask:0xf\n"
        "v_mfma_f32_16x16x32_bf16 v[48:51], a[252:255], v[224:227], v[48:51]\n"
        "ds_read_b64_tr_b16 a[238:239], v43 offset:57344\n"
        "v_cndmask_b32_dpp v236, v243, v241, vcc row_ror:8 row_mask:0xf bank_mask:0xf\n"
        "v_mfma_f32_16x16x32_bf16 v[52:55], a[252:255], v[228:231], v[52:55]\n"
        "ds_read_b64_tr_b16 a[240:241], v19 offset:57344\n"
        "v_cndmask_b32_dpp v238, v247, v245, vcc row_ror:8 row_mask:0xf bank_mask:0xf\n"
        "v_mfma_f32_16x16x32_bf16 v[56:59], a[224:227], v[224:227], v[56:59]\n"
        "ds_read_b64_tr_b16 a[242:243], v44 offset:57344\n"
        "s_mov_b64 vcc, s[56:57]\n"
        "v_mfma_f32_16x16x32_bf16 v[60:63], a[224:227], v[228:231], v[60:63]\n"
        "v_cndmask_b32_dpp v233, v240, v242, vcc row_ror:8 row_mask:0xf bank_mask:0xf\n"
        "v_cndmask_b32_dpp v235, v244, v246, vcc row_ror:8 row_mask:0xf bank_mask:0xf\n"
        "v_mfma_f32_16x16x32_bf16 v[64:67], a[228:231], v[224:227], v[64:67]\n"
        "buffer_load_dwordx4 a[116:119], v102, s[36:39], s93 offen offset:1024 sc1\n"
        "v_cndmask_b32_dpp v237, v241, v243, vcc row_ror:8 row_mask:0xf bank_mask:0xf\n"
        "v_mfma_f32_16x16x32_bf16 v[68:71], a[228:231], v[228:231], v[68:71]\n"
        "buffer_load_dwordx4 a[120:123], v102, s[36:39], s93 offen offset:2048 sc1\n"
        "v_cndmask_b32_dpp v239, v245, v247, vcc row_ror:8 row_mask:0xf bank_mask:0xf\n"
        "v_mfma_f32_16x16x32_bf16 v[104:107], v[232:235], v[232:235], v[104:107]\n"
        "ds_read_b64_tr_b16 a[244:245], v13 offset:61440\n"
        "v_cndmask_b32_dpp v241, v248, v250, vcc row_ror:8 row_mask:0xf bank_mask:0xf\n"
        "v_cndmask_b32_dpp v243, v252, v254, vcc row_ror:8 row_mask:0xf bank_mask:0xf\n"
        "v_mfma_f32_16x16x32_bf16 v[112:115], v[76:79], v[232:235], v[112:115]\n"
        "ds_read_b64_tr_b16 a[246:247], v42 offset:61440\n"
        "v_cndmask_b32_dpp v245, v249, v251, vcc row_ror:8 row_mask:0xf bank_mask:0xf\n"
        "v_mfma_f32_16x16x32_bf16 v[108:111], v[236:239], v[236:239], v[108:111]\n"
        "ds_read_b64_tr_b16 a[248:249], v14 offset:61440\n"
        "v_cndmask_b32_dpp v247, v253, v255, vcc row_ror:8 row_mask:0xf bank_mask:0xf\n"
        "s_mov_b64 vcc, s[54:55]\n"
        "v_mfma_f32_16x16x32_bf16 v[116:119], v[76:79], v[236:239], v[116:119]\n"
        "s_waitcnt lgkmcnt(3)\n"
        "v_mfma_f32_16x16x32_bf16 v[48:51], a[232:235], v[232:235], v[48:51]\n"
        "ds_read_b64_tr_b16 a[250:251], v43 offset:61440\n"
        "v_cndmask_b32_dpp v240, v250, v248, vcc row_ror:8 row_mask:0xf bank_mask:0xf\n"
        "v_mfma_f32_16x16x32_bf16 v[52:55], a[232:235], v[236:239], v[52:55]\n"
        "ds_read_b64_tr_b16 a[252:253], v19 offset:61440\n"
        "v_mfma_f32_16x16x32_bf16 v[56:59], a[236:239], v[232:235], v[56:59]\n"
        "ds_read_b64_tr_b16 a[254:255], v44 offset:61440\n"
        "v_cndmask_b32_dpp v242, v254, v252, vcc row_ror:8 row_mask:0xf bank_mask:0xf\n"
        "v_mfma_f32_16x16x32_bf16 v[60:63], a[236:239], v[236:239], v[60:63]\n"
        "v_cndmask_b32_dpp v244, v251, v249, vcc row_ror:8 row_mask:0xf bank_mask:0xf\n"
        "v_mfma_f32_16x16x32_bf16 v[64:67], a[240:243], v[232:235], v[64:67]\n"
        "buffer_load_dwordx4 a[164:167], v103, s[36:39], s92 offen offset:1024 sc1\n"
        "v_cndmask_b32_dpp v246, v255, v253, vcc row_ror:8 row_mask:0xf bank_mask:0xf\n"
        "v_mfma_f32_16x16x32_bf16 v[68:71], a[240:243], v[236:239], v[68:71]\n"
        "buffer_load_dwordx4 a[124:127], v102, s[36:39], s93 offen offset:3072 sc1\n"
        "v_add_u32 v5, 0x10000, v15\n"
        "v_add_u32 v6, 0x10000, v16\n"
        "v_mfma_f32_16x16x32_bf16 v[104:107], v[240:243], v[240:243], v[104:107]\n"
        "ds_read_b64_tr_b16 a[224:225], v15\n"
        "s_lshr_b32 s52, s41, 3\n"
        "v_mad_u32_u24 v250, v3, 3, 3\n"
        "v_mfma_f32_16x16x32_bf16 v[112:115], v[76:79], v[240:243], v[112:115]\n"
        "ds_read_b64_tr_b16 a[226:227], v5\n"
        "v_lshrrev_b32_e64 v250, v250, s52\n"
        "v_mfma_f32_16x16x32_bf16 v[108:111], v[244:247], v[244:247], v[108:111]\n"
        "ds_read_b64_tr_b16 a[228:229], v16\n"
        "v_add_u32 v250, s52, v250\n"
        "v_and_b32 v248, 1, v250\n"
        "v_mfma_f32_16x16x32_bf16 v[116:119], v[76:79], v[244:247], v[116:119]\n"
        "s_waitcnt lgkmcnt(3)\n"
        "v_lshlrev_b32 v248, 4, v248\n"
        "v_mfma_f32_16x16x32_bf16 v[48:51], a[244:247], v[240:243], v[48:51]\n"
        "ds_read_b64_tr_b16 a[230:231], v6\n"
        "v_bfrev_b32 v249, 1\n"
        "v_lshrrev_b32 v248, v248, v249\n"
        "v_mfma_f32_16x16x32_bf16 v[52:55], a[244:247], v[244:247], v[52:55]\n"
        "ds_read_b64_tr_b16 a[232:233], v15 offset:4096\n"
        "v_lshlrev_b32 v249, 29, v250\n"
        "v_mfma_f32_16x16x32_bf16 v[56:59], a[248:251], v[240:243], v[56:59]\n"
        "ds_read_b64_tr_b16 a[234:235], v5 offset:4096\n"
        "v_bfe_u32 v250, v250, 1, 1\n"
        "v_mfma_f32_16x16x32_bf16 v[60:63], a[248:251], v[244:247], v[60:63]\n"
        "ds_read_b64_tr_b16 a[236:237], v16 offset:4096\n"
        "v_and_b32 v249, 0x80000000, v249\n"
        "v_mfma_f32_16x16x32_bf16 v[64:67], a[252:255], v[240:243], v[64:67]\n"
        "ds_read_b64_tr_b16 a[238:239], v6 offset:4096\n"
        "buffer_load_dwordx4 a[128:131], v102, s[36:39], s94 offen sc1\n"
        "v_mfma_f32_16x16x32_bf16 v[68:71], a[252:255], v[244:247], v[68:71]\n"
        "buffer_load_dwordx4 a[132:135], v102, s[36:39], s94 offen offset:1024 sc1\n"
        "v_lshl_or_b32 v249, v250, 15, v249\n"
        "s_waitcnt lgkmcnt(4)\n"
        "v_mfma_f32_16x16x32_bf16 v[32:35], a[224:227], v[120:123], 0\n"
        "buffer_load_dwordx4 a[168:171], v103, s[36:39], s92 offen offset:2048 sc1\n"
        "ds_read_b64_tr_b16 a[240:241], v15 offset:8192\n"
        "ds_read_b64_tr_b16 a[242:243], v5 offset:8192\n"
        "v_cndmask_b32_e64 v24, 0, v104, s[10:11]\n"
        "v_cndmask_b32_e64 v24, v24, v105, s[12:13]\n"
        "v_cndmask_b32_e64 v24, v24, v106, s[14:15]\n"
        "v_mfma_f32_16x16x32_bf16 v[36:39], a[224:227], v[124:127], 0\n"
        "ds_read_b64_tr_b16 a[244:245], v16 offset:8192\n"
        "ds_read_b64_tr_b16 a[246:247], v6 offset:8192\n"
        "v_cndmask_b32_e64 v24, v24, v107, s[16:17]\n"
        "v_cndmask_b32_e64 v25, 0, v108, s[10:11]\n"
        "v_mfma_f32_16x16x32_bf16 v[40:43], a[228:231], v[120:123], 0\n"
        "ds_read_b64_tr_b16 a[248:249], v15 offset:12288\n"
        "ds_read_b64_tr_b16 a[250:251], v5 offset:12288\n"
        "v_cndmask_b32_e64 v25, v25, v109, s[12:13]\n"
        "v_cndmask_b32_e64 v25, v25, v110, s[14:15]\n"
        "v_mfma_f32_16x16x32_bf16 v[44:47], a[228:231], v[124:127], 0\n"
        "v_cndmask_b32_e64 v25, v25, v111, s[16:17]\n"
        "v_permlane32_swap_b32 v112, v116\n"
        "s_mov_b32 s49, 0x3b000000\n"
        "ds_read_b64_tr_b16 a[252:253], v16 offset:12288\n"
        "ds_read_b64_tr_b16 a[254:255], v6 offset:12288\n"
        "s_waitcnt lgkmcnt(8)\n"
        "v_mfma_f32_16x16x32_bf16 v[32:35], a[232:235], v[128:131], v[32:35]\n"
        "buffer_load_dwordx4 a[136:139], v102, s[36:39], s94 offen offset:2048 sc1\n"
        "ds_read_b64_tr_b16 a[224:225], v15 offset:16384\n"
        "v_permlane32_swap_b32 v24, v25\n"
        "v_mul_f32 v26, s49, v112\n"
        "v_mfma_f32_16x16x32_bf16 v[36:39], a[232:235], v[132:135], v[36:39]\n"
        "ds_read_b64_tr_b16 a[226:227], v5 offset:16384\n"
        "v_add_f32 v24, v24, v25\n"
        "v_mov_b32 v25, v24\n"
        "v_mfma_f32_16x16x32_bf16 v[40:43], a[236:239], v[128:131], v[40:43]\n"
        "ds_read_b64_tr_b16 a[228:229], v16 offset:16384\n"
        "v_cvt_pk_bf16_f32 v80, -v26, -v26\n"
        "v_mul_f32 v26, v26, v26\n"
        "v_mfma_f32_16x16x32_bf16 v[44:47], a[236:239], v[132:135], v[44:47]\n"
        "ds_read_b64_tr_b16 a[230:231], v6 offset:16384\n"
        "v_permlane16_swap_b32 v24, v25\n"
        "v_mov_b32 v84, v80\n"
        "v_add_f32 v24, v24, v25\n"
        "s_waitcnt lgkmcnt(8)\n"
        "v_mfma_f32_16x16x32_bf16 v[32:35], a[240:243], v[136:139], v[32:35]\n"
        "buffer_load_dwordx4 a[140:143], v102, s[36:39], s94 offen offset:3072 sc1\n"
        "ds_read_b64_tr_b16 a[232:233], v15 offset:20480\n"
        "v_fma_f32 v24, v24, s49, -v26\n"
        "v_max_f32 v24, 0, v24\n"
        "v_mfma_f32_16x16x32_bf16 v[36:39], a[240:243], v[140:143], v[36:39]\n"
        "ds_read_b64_tr_b16 a[234:235], v5 offset:20480\n"
        "v_cvt_pk_bf16_f32 v24, v24, v24\n"
        "v_lshlrev_b32 v24, 16, v24\n"
        "v_mfma_f32_16x16x32_bf16 v[40:43], a[244:247], v[136:139], v[40:43]\n"
        "ds_read_b64_tr_b16 a[236:237], v16 offset:20480\n"
        "v_add_f32 v24, 0x3727c5ac, v24\n"
        "v_cvt_pk_bf16_f32 v24, v24, v24\n"
        "v_mfma_f32_16x16x32_bf16 v[44:47], a[244:247], v[140:143], v[44:47]\n"
        "ds_read_b64_tr_b16 a[238:239], v6 offset:20480\n"
        "v_lshlrev_b32 v24, 16, v24\n"
        "v_sqrt_f32 v24, v24\n"
        "v_permlane32_swap_b32 v80, v84\n"
        "s_waitcnt lgkmcnt(8)\n"
        "v_mfma_f32_16x16x32_bf16 v[32:35], a[248:251], v[144:147], v[32:35]\n"
        "buffer_load_dwordx4 a[144:147], v102, s[36:39], s95 offen sc1\n"
        "ds_read_b64_tr_b16 a[240:241], v15 offset:24576\n"
        "v_cvt_pk_bf16_f32 v81, v24, 0\n"
        "v_mov_b32 v85, v81\n"
        "v_mfma_f32_16x16x32_bf16 v[36:39], a[248:251], v[148:151], v[36:39]\n"
        "ds_read_b64_tr_b16 a[242:243], v5 offset:24576\n"
        "v_lshlrev_b32 v24, 16, v81\n"
        "v_rcp_f32 v24, v24\n"
        "v_mfma_f32_16x16x32_bf16 v[40:43], a[252:255], v[144:147], v[40:43]\n"
        "ds_read_b64_tr_b16 a[244:245], v16 offset:24576\n"
        "v_permlane32_swap_b32 v81, v85\n"
        "v_mov_b64 v[72:73], v[88:89]\n"
        "v_mfma_f32_16x16x32_bf16 v[44:47], a[252:255], v[148:151], v[44:47]\n"
        "v_mul_f32 v76, v24, v24\n"
        "v_mov_b32 v77, s41\n"
        "v_mfma_f32_16x16x32_bf16 v[48:51], v[72:75], v[80:83], v[48:51]\n"
        "ds_read_b64_tr_b16 a[246:247], v6 offset:24576\n"
        "s_waitcnt lgkmcnt(8)\n"
        "v_mfma_f32_16x16x32_bf16 v[32:35], a[224:227], v[152:155], v[32:35]\n"
        "v_mfma_f32_16x16x32_bf16 v[52:55], v[72:75], v[84:87], v[52:55]\n"
        "ds_read_b64_tr_b16 a[248:249], v15 offset:28672\n"
        "v_mov_b64 v[72:73], v[90:91]\n"
        "v_mfma_f32_16x16x32_bf16 v[36:39], a[224:227], v[156:159], v[36:39]\n"
        "s_nop 0\n"
        "v_mfma_f32_16x16x32_bf16 v[56:59], v[72:75], v[80:83], v[56:59]\n"
        "v_mfma_f32_16x16x32_bf16 v[60:63], v[72:75], v[84:87], v[60:63]\n"
        "buffer_load_dwordx4 a[172:175], v103, s[36:39], s92 offen offset:3072 sc1\n"
        "ds_read_b64_tr_b16 a[250:251], v5 offset:28672\n"
        "v_mfma_f32_16x16x32_bf16 v[40:43], a[228:231], v[152:155], v[40:43]\n"
        "v_mov_b64 v[72:73], v[100:101]\n"
        "s_nop 1\n"
        "v_mfma_f32_16x16x32_bf16 v[64:67], v[72:75], v[80:83], v[64:67]\n"
        "v_mfma_f32_16x16x32_bf16 v[68:71], v[72:75], v[84:87], v[68:71]\n"
        "ds_read_b64_tr_b16 a[252:253], v16 offset:28672\n"
        "v_mfma_f32_16x16x32_bf16 v[44:47], a[228:231], v[156:159], v[44:47]\n"
        "ds_read_b64_tr_b16 a[254:255], v6 offset:28672\n"
        "v_mov_b64 v[72:73], v[92:93]\n"
        "v_mul_f32_e64 v48, v48, |v48|\n"
        "s_waitcnt lgkmcnt(8)\n"
        "v_mfma_f32_16x16x32_bf16 v[32:35], a[232:235], v[160:163], v[32:35]\n"
        "ds_read_b64_tr_b16 a[224:225], v15 offset:32768\n"
        "v_mul_f32_e64 v49, v49, |v49|\n"
        "v_mul_f32_e64 v50, v50, |v50|\n"
        "v_mfma_f32_16x16x32_bf16 v[36:39], a[232:235], v[164:167], v[36:39]\n"
        "buffer_load_dwordx4 a[148:151], v102, s[36:39], s95 offen offset:1024 sc1\n"
        "ds_read_b64_tr_b16 a[226:227], v5 offset:32768\n"
        "v_mul_f32_e64 v51, v51, |v51|\n"
        "v_cvt_pk_bf16_f32 v24, v48, v49\n"
        "v_mfma_f32_16x16x32_bf16 v[40:43], a[236:239], v[160:163], v[40:43]\n"
        "ds_read_b64_tr_b16 a[228:229], v16 offset:32768\n"
        "v_cvt_pk_bf16_f32 v25, v50, v51\n"
        "v_pk_max_i16 v24, v24, 0\n"
        "v_pk_max_i16 v25, v25, 0\n"
        "v_mfma_f32_16x16x32_bf16 v[44:47], a[236:239], v[164:167], v[44:47]\n"
        "ds_read_b64_tr_b16 a[230:231], v6 offset:32768\n"
        "v_or_b32 v24, v248, v24\n"
        "v_or_b32 v25, v249, v25\n"
        "s_waitcnt lgkmcnt(8)\n"
        "v_mfma_f32_16x16x32_bf16 v[32:35], a[240:243], v[168:171], v[32:35]\n"
        "ds_read_b64_tr_b16 a[232:233], v15 offset:36864\n"
        "v_mul_f32_e64 v52, v52, |v52|\n"
        "v_mul_f32_e64 v53, v53, |v53|\n"
        "s_cbranch_execz .LWARM_RET_%=\n"
        "v_mfma_f32_16x16x32_bf16 v[36:39], a[240:243], v[172:175], v[36:39]\n"
        "buffer_load_dwordx4 a[152:155], v102, s[36:39], s95 offen offset:2048 sc1\n"
        "ds_read_b64_tr_b16 a[234:235], v5 offset:36864\n"
        "v_mul_f32_e64 v54, v54, |v54|\n"
        "v_mul_f32_e64 v55, v55, |v55|\n"
        "v_mfma_f32_16x16x32_bf16 v[40:43], a[244:247], v[168:171], v[40:43]\n"
        "ds_read_b64_tr_b16 a[236:237], v16 offset:36864\n"
        "v_cvt_pk_bf16_f32 v28, v52, v53\n"
        "v_cvt_pk_bf16_f32 v29, v54, v55\n"
        "v_pk_max_i16 v28, v28, 0\n"
        "v_mfma_f32_16x16x32_bf16 v[44:47], a[244:247], v[172:175], v[44:47]\n"
        "ds_read_b64_tr_b16 a[238:239], v6 offset:36864\n"
        "v_pk_max_i16 v29, v29, 0\n"
        "v_or_b32 v28, v248, v28\n"
        "s_waitcnt lgkmcnt(8)\n"
        "v_mfma_f32_16x16x32_bf16 v[32:35], a[248:251], v[176:179], v[32:35]\n"
        "ds_read_b64_tr_b16 a[240:241], v15 offset:40960\n"
        "v_or_b32 v29, v249, v29\n"
        "v_mul_f32_e64 v56, v56, |v56|\n"
        "v_mfma_f32_16x16x32_bf16 v[36:39], a[248:251], v[180:183], v[36:39]\n"
        "ds_read_b64_tr_b16 a[242:243], v5 offset:40960\n"
        "v_mul_f32_e64 v57, v57, |v57|\n"
        "v_mul_f32_e64 v58, v58, |v58|\n"
        "v_mfma_f32_16x16x32_bf16 v[40:43], a[252:255], v[176:179], v[40:43]\n"
        "buffer_load_dwordx4 a[156:159], v102, s[36:39], s95 offen offset:3072 sc1\n"
        "ds_read_b64_tr_b16 a[244:245], v16 offset:40960\n"
        "v_mul_f32_e64 v59, v59, |v59|\n"
        "v_cvt_pk_bf16_f32 v26, v56, v57\n"
        "v_cvt_pk_bf16_f32 v27, v58, v59\n"
        "v_mfma_f32_16x16x32_bf16 v[44:47], a[252:255], v[180:183], v[44:47]\n"
        "ds_read_b64_tr_b16 a[246:247], v6 offset:40960\n"
        "v_pk_max_i16 v26, v26, 0\n"
        "v_pk_max_i16 v27, v27, 0\n"
        "s_waitcnt lgkmcnt(8)\n"
        "v_mfma_f32_16x16x32_bf16 v[32:35], a[224:227], v[184:187], v[32:35]\n"
        "ds_read_b64_tr_b16 a[248:249], v15 offset:45056\n"
        "v_or_b32 v26, v248, v26\n"
        "v_or_b32 v27, v249, v27\n"
        "v_mfma_f32_16x16x32_bf16 v[36:39], a[224:227], v[188:191], v[36:39]\n"
        "ds_read_b64_tr_b16 a[250:251], v5 offset:45056\n"
        "v_mul_f32_e64 v60, v60, |v60|\n"
        "v_mul_f32_e64 v61, v61, |v61|\n"
        "v_mul_f32_e64 v62, v62, |v62|\n"
        "v_mfma_f32_16x16x32_bf16 v[40:43], a[228:231], v[184:187], v[40:43]\n"
        "buffer_load_dwordx4 a[176:179], v103, s[36:39], s93 offen sc1\n"
        "ds_read_b64_tr_b16 a[252:253], v16 offset:45056\n"
        "v_mul_f32_e64 v63, v63, |v63|\n"
        "v_cvt_pk_bf16_f32 v30, v60, v61\n"
        "v_mfma_f32_16x16x32_bf16 v[44:47], a[228:231], v[188:191], v[44:47]\n"
        "ds_read_b64_tr_b16 a[254:255], v6 offset:45056\n"
        "v_cvt_pk_bf16_f32 v31, v62, v63\n"
        "v_pk_max_i16 v30, v30, 0\n"
        "s_waitcnt lgkmcnt(8)\n"
        "v_mfma_f32_16x16x32_bf16 v[32:35], a[232:235], v[192:195], v[32:35]\n"
        "ds_read_b64_tr_b16 a[224:225], v15 offset:49152\n"
        "v_pk_max_i16 v31, v31, 0\n"
        "v_or_b32 v30, v248, v30\n"
        "v_mfma_f32_16x16x32_bf16 v[36:39], a[232:235], v[196:199], v[36:39]\n"
        "ds_read_b64_tr_b16 a[226:227], v5 offset:49152\n"
        "v_or_b32 v31, v249, v31\n"
        "v_mul_f32_e64 v64, v64, |v64|\n"
        "v_mul_f32_e64 v65, v65, |v65|\n"
        "v_mfma_f32_16x16x32_bf16 v[40:43], a[236:239], v[192:195], v[40:43]\n"
        "buffer_load_dwordx4 a[180:183], v103, s[36:39], s93 offen offset:1024 sc1\n"
        "ds_read_b64_tr_b16 a[228:229], v16 offset:49152\n"
        "v_mul_f32_e64 v66, v66, |v66|\n"
        "v_mul_f32_e64 v67, v67, |v67|\n"
        "v_mfma_f32_16x16x32_bf16 v[44:47], a[236:239], v[196:199], v[44:47]\n"
        "ds_read_b64_tr_b16 a[230:231], v6 offset:49152\n"
        "v_cvt_pk_bf16_f32 v48, v64, v65\n"
        "v_cvt_pk_bf16_f32 v49, v66, v67\n"
        "s_waitcnt lgkmcnt(8)\n"
        "v_mfma_f32_16x16x32_bf16 v[32:35], a[240:243], v[200:203], v[32:35]\n"
        "ds_read_b64_tr_b16 a[232:233], v15 offset:53248\n"
        "v_pk_max_i16 v48, v48, 0\n"
        "v_pk_max_i16 v49, v49, 0\n"
        "v_mfma_f32_16x16x32_bf16 v[36:39], a[240:243], v[204:207], v[36:39]\n"
        "ds_read_b64_tr_b16 a[234:235], v5 offset:53248\n"
        "v_or_b32 v48, v248, v48\n"
        "v_or_b32 v49, v249, v49\n"
        "v_mul_f32_e64 v68, v68, |v68|\n"
        "v_mfma_f32_16x16x32_bf16 v[40:43], a[244:247], v[200:203], v[40:43]\n"
        "ds_read_b64_tr_b16 a[236:237], v16 offset:53248\n"
        "v_mul_f32_e64 v69, v69, |v69|\n"
        "v_mul_f32_e64 v70, v70, |v70|\n"
        "v_mfma_f32_16x16x32_bf16 v[44:47], a[244:247], v[204:207], v[44:47]\n"
        "ds_read_b64_tr_b16 a[238:239], v6 offset:53248\n"
        "v_mul_f32_e64 v71, v71, |v71|\n"
        "v_cvt_pk_bf16_f32 v50, v68, v69\n"
        "s_waitcnt lgkmcnt(8)\n"
        "v_mfma_f32_16x16x32_bf16 v[32:35], a[248:251], v[208:211], v[32:35]\n"
        "ds_read_b64_tr_b16 a[240:241], v15 offset:57344\n"
        "v_cvt_pk_bf16_f32 v51, v70, v71\n"
        "v_pk_max_i16 v50, v50, 0\n"
        "v_mfma_f32_16x16x32_bf16 v[36:39], a[248:251], v[212:215], v[36:39]\n"
        "ds_read_b64_tr_b16 a[242:243], v5 offset:57344\n"
        "v_pk_max_i16 v51, v51, 0\n"
        "v_or_b32 v50, v248, v50\n"
        "v_or_b32 v51, v249, v51\n"
        "v_mfma_f32_16x16x32_bf16 v[40:43], a[252:255], v[208:211], v[40:43]\n"
        "buffer_store_dwordx2 v[76:77], v11, s[36:39], 0 offen sc1\n"
        "buffer_store_dwordx4 v[48:51], v4, s[36:39], s3 offen sc1\n"
        "ds_read_b64_tr_b16 a[244:245], v16 offset:57344\n"
        "v_mfma_f32_16x16x32_bf16 v[44:47], a[252:255], v[212:215], v[44:47]\n"
        "s_add_u32 s49, s62, 0\n"
        "s_add_u32 s50, s62, 0xe000\n"
        "ds_read_b64_tr_b16 a[246:247], v6 offset:57344\n"
        "s_waitcnt lgkmcnt(8)\n"
        "v_mfma_f32_16x16x32_bf16 v[32:35], a[224:227], v[216:219], v[32:35]\n"
        "buffer_store_dwordx4 v[24:27], v4, s[36:39], s49 offen sc1\n"
        "buffer_store_dwordx4 v[28:31], v4, s[36:39], s50 offen sc1\n"
        "ds_read_b64_tr_b16 a[248:249], v15 offset:61440\n"
        "v_mfma_f32_16x16x32_bf16 v[36:39], a[224:227], v[220:223], v[36:39]\n"
        "ds_read_b64_tr_b16 a[250:251], v5 offset:61440\n"
        "v_mfma_f32_16x16x32_bf16 v[40:43], a[228:231], v[216:219], v[40:43]\n"
        "buffer_load_dwordx4 a[184:187], v103, s[36:39], s93 offen offset:2048 sc1\n"
        "ds_read_b64_tr_b16 a[252:253], v16 offset:61440\n"
        "v_mfma_f32_16x16x32_bf16 v[44:47], a[228:231], v[220:223], v[44:47]\n"
        "ds_read_b64_tr_b16 a[254:255], v6 offset:61440\n"
        "v_add_u32 v13, 0x10000, v17\n"
        "v_add_u32 v14, 0x10000, v18\n"
        "s_waitcnt lgkmcnt(8)\n"
        "v_mfma_f32_16x16x32_bf16 v[32:35], a[232:235], v[224:227], v[32:35]\n"
        "ds_read_b64_tr_b16 a[224:225], v17\n"
        "v_mfma_f32_16x16x32_bf16 v[36:39], a[232:235], v[228:231], v[36:39]\n"
        "buffer_load_dwordx4 a[188:191], v103, s[36:39], s93 offen offset:3072 sc1\n"
        "ds_read_b64_tr_b16 a[226:227], v13\n"
        "v_mfma_f32_16x16x32_bf16 v[40:43], a[236:239], v[224:227], v[40:43]\n"
        "ds_read_b64_tr_b16 a[228:229], v18\n"
        "v_mfma_f32_16x16x32_bf16 v[44:47], a[236:239], v[228:231], v[44:47]\n"
        "ds_read_b64_tr_b16 a[230:231], v14\n"
        "s_waitcnt lgkmcnt(8)\n"
        "v_mfma_f32_16x16x32_bf16 v[32:35], a[240:243], v[232:235], v[32:35]\n"
        "ds_read_b64_tr_b16 a[232:233], v17 offset:4096\n"
        "v_mfma_f32_16x16x32_bf16 v[36:39], a[240:243], v[236:239], v[36:39]\n"
        "buffer_load_dwordx4 a[192:195], v103, s[36:39], s94 offen sc1\n"
        "ds_read_b64_tr_b16 a[234:235], v13 offset:4096\n"
        "v_mfma_f32_16x16x32_bf16 v[40:43], a[244:247], v[232:235], v[40:43]\n"
        "ds_read_b64_tr_b16 a[236:237], v18 offset:4096\n"
        "v_mfma_f32_16x16x32_bf16 v[44:47], a[244:247], v[236:239], v[44:47]\n"
        "ds_read_b64_tr_b16 a[238:239], v14 offset:4096\n"
        "s_waitcnt lgkmcnt(8)\n"
        "v_mfma_f32_16x16x32_bf16 v[32:35], a[248:251], v[240:243], v[32:35]\n"
        "buffer_load_dwordx4 a[196:199], v103, s[36:39], s94 offen offset:1024 sc1\n"
        "ds_read_b64_tr_b16 a[240:241], v17 offset:8192\n"
        "v_mfma_f32_16x16x32_bf16 v[36:39], a[248:251], v[244:247], v[36:39]\n"
        "ds_read_b64_tr_b16 a[242:243], v13 offset:8192\n"
        "v_mfma_f32_16x16x32_bf16 v[40:43], a[252:255], v[240:243], v[40:43]\n"
        "ds_read_b64_tr_b16 a[244:245], v18 offset:8192\n"
        "v_mfma_f32_16x16x32_bf16 v[44:47], a[252:255], v[244:247], v[44:47]\n"
        "ds_read_b64_tr_b16 a[246:247], v14 offset:8192\n"
        "s_waitcnt lgkmcnt(8)\n"
        "v_mfma_f32_16x16x32_bf16 v[104:107], a[224:227], v[120:123], 0\n"
        "v_mfma_f32_16x16x32_bf16 v[32:35], v[72:75], v[80:83], v[32:35]\n"
        "v_mfma_f32_16x16x32_bf16 v[36:39], v[72:75], v[84:87], v[36:39]\n"
        "buffer_load_dwordx4 a[200:203], v103, s[36:39], s94 offen offset:2048 sc1\n"
        "ds_read_b64_tr_b16 a[248:249], v17 offset:12288\n"
        "v_mfma_f32_16x16x32_bf16 v[108:111], a[224:227], v[124:127], 0\n"
        "v_mov_b64 v[72:73], v[94:95]\n"
        "s_nop 1\n"
        "v_mfma_f32_16x16x32_bf16 v[40:43], v[72:75], v[80:83], v[40:43]\n"
        "ds_read_b64_tr_b16 a[250:251], v13 offset:12288\n"
        "v_mfma_f32_16x16x32_bf16 v[112:115], a[228:231], v[120:123], 0\n"
        "v_mfma_f32_16x16x32_bf16 v[44:47], v[72:75], v[84:87], v[44:47]\n"
        "ds_read_b64_tr_b16 a[252:253], v18 offset:12288\n"
        "v_mfma_f32_16x16x32_bf16 v[116:119], a[228:231], v[124:127], 0\n"
        "buffer_load_dwordx4 a[204:207], v103, s[36:39], s94 offen offset:3072 sc1\n"
        "ds_read_b64_tr_b16 a[254:255], v14 offset:12288\n"
        "v_mov_b64 v[72:73], v[96:97]\n"
        "v_mul_f32_e64 v32, v32, |v32|\n"
        "s_waitcnt lgkmcnt(8)\n"
        "v_mfma_f32_16x16x32_bf16 v[104:107], a[232:235], v[128:131], v[104:107]\n"
        "ds_read_b64_tr_b16 a[224:225], v17 offset:16384\n"
        "v_mul_f32_e64 v33, v33, |v33|\n"
        "v_mul_f32_e64 v34, v34, |v34|\n"
        "v_mfma_f32_16x16x32_bf16 v[108:111], a[232:235], v[132:135], v[108:111]\n"
        "ds_read_b64_tr_b16 a[226:227], v13 offset:16384\n"
        "v_mul_f32_e64 v35, v35, |v35|\n"
        "v_mfma_f32_16x16x32_bf16 v[112:115], a[236:239], v[128:131], v[112:115]\n"
        "ds_read_b64_tr_b16 a[228:229], v18 offset:16384\n"
        "v_cvt_pk_bf16_f32 v24, v32, v33\n"
        "v_cvt_pk_bf16_f32 v25, v34, v35\n"
        "v_mfma_f32_16x16x32_bf16 v[116:119], a[236:239], v[132:135], v[116:119]\n"
        "buffer_load_dwordx4 a[208:211], v103, s[36:39], s95 offen sc1\n"
        "ds_read_b64_tr_b16 a[230:231], v14 offset:16384\n"
        "v_pk_max_i16 v24, v24, 0\n"
        "s_waitcnt lgkmcnt(8)\n"
        "v_mfma_f32_16x16x32_bf16 v[104:107], a[240:243], v[136:139], v[104:107]\n"
        "ds_read_b64_tr_b16 a[232:233], v17 offset:20480\n"
        "v_pk_max_i16 v25, v25, 0\n"
        "v_or_b32 v24, v248, v24\n"
        "v_mfma_f32_16x16x32_bf16 v[108:111], a[240:243], v[140:143], v[108:111]\n"
        "ds_read_b64_tr_b16 a[234:235], v13 offset:20480\n"
        "v_or_b32 v25, v249, v25\n"
        "v_mul_f32_e64 v36, v36, |v36|\n"
        "v_mfma_f32_16x16x32_bf16 v[112:115], a[244:247], v[136:139], v[112:115]\n"
        "ds_read_b64_tr_b16 a[236:237], v18 offset:20480\n"
        "v_mul_f32_e64 v37, v37, |v37|\n"
        "v_mfma_f32_16x16x32_bf16 v[116:119], a[244:247], v[140:143], v[116:119]\n"
        "buffer_load_dwordx4 a[212:215], v103, s[36:39], s95 offen offset:1024 sc1\n"
        "ds_read_b64_tr_b16 a[238:239], v14 offset:20480\n"
        "v_mul_f32_e64 v38, v38, |v38|\n"
        "v_mul_f32_e64 v39, v39, |v39|\n"
        "s_waitcnt lgkmcnt(8)\n"
        "v_mfma_f32_16x16x32_bf16 v[104:107], a[248:251], v[144:147], v[104:107]\n"
        "ds_read_b64_tr_b16 a[240:241], v17 offset:24576\n"
        "v_cvt_pk_bf16_f32 v28, v36, v37\n"
        "v_mfma_f32_16x16x32_bf16 v[108:111], a[248:251], v[148:151], v[108:111]\n"
        "ds_read_b64_tr_b16 a[242:243], v13 offset:24576\n"
        "v_cvt_pk_bf16_f32 v29, v38, v39\n"
        "v_pk_max_i16 v28, v28, 0\n"
        "v_mfma_f32_16x16x32_bf16 v[112:115], a[252:255], v[144:147], v[112:115]\n"
        "ds_read_b64_tr_b16 a[244:245], v18 offset:24576\n"
        "v_pk_max_i16 v29, v29, 0\n"
        "v_or_b32 v28, v248, v28\n"
        "v_mfma_f32_16x16x32_bf16 v[116:119], a[252:255], v[148:151], v[116:119]\n"
        "buffer_load_dwordx4 a[216:219], v103, s[36:39], s95 offen offset:2048 sc1\n"
        "ds_read_b64_tr_b16 a[246:247], v14 offset:24576\n"
        "v_or_b32 v29, v249, v29\n"
        "s_waitcnt lgkmcnt(8)\n"
        "v_mfma_f32_16x16x32_bf16 v[104:107], a[224:227], v[152:155], v[104:107]\n"
        "ds_read_b64_tr_b16 a[248:249], v17 offset:28672\n"
        "v_mul_f32_e64 v40, v40, |v40|\n"
        "v_mul_f32_e64 v41, v41, |v41|\n"
        "v_mfma_f32_16x16x32_bf16 v[108:111], a[224:227], v[156:159], v[108:111]\n"
        "ds_read_b64_tr_b16 a[250:251], v13 offset:28672\n"
        "v_mul_f32_e64 v42, v42, |v42|\n"
        "v_mul_f32_e64 v43, v43, |v43|\n"
        "v_mfma_f32_16x16x32_bf16 v[112:115], a[228:231], v[152:155], v[112:115]\n"
        "ds_read_b64_tr_b16 a[252:253], v18 offset:28672\n"
        "v_cvt_pk_bf16_f32 v26, v40, v41\n"
        "v_mfma_f32_16x16x32_bf16 v[116:119], a[228:231], v[156:159], v[116:119]\n"
        "buffer_load_dwordx4 a[220:223], v103, s[36:39], s95 offen offset:3072 sc1\n"
        "ds_read_b64_tr_b16 a[254:255], v14 offset:28672\n"
        "v_cvt_pk_bf16_f32 v27, v42, v43\n"
        "v_pk_max_i16 v26, v26, 0\n"
        "s_waitcnt lgkmcnt(8)\n"
        "v_mfma_f32_16x16x32_bf16 v[104:107], a[232:235], v[160:163], v[104:107]\n"
        "ds_read_b64_tr_b16 a[224:225], v17 offset:32768\n"
        "v_pk_max_i16 v27, v27, 0\n"
        "v_mfma_f32_16x16x32_bf16 v[108:111], a[232:235], v[164:167], v[108:111]\n"
        "ds_read_b64_tr_b16 a[226:227], v13 offset:32768\n"
        "v_or_b32 v26, v248, v26\n"
        "v_or_b32 v27, v249, v27\n"
        "v_mfma_f32_16x16x32_bf16 v[112:115], a[236:239], v[160:163], v[112:115]\n"
        "ds_read_b64_tr_b16 a[228:229], v18 offset:32768\n"
        "v_mul_f32_e64 v44, v44, |v44|\n"
        "v_mul_f32_e64 v45, v45, |v45|\n"
        "v_mfma_f32_16x16x32_bf16 v[116:119], a[236:239], v[164:167], v[116:119]\n"
        "buffer_load_dwordx4 v[64:67], v0, s[36:39], s63 offen sc1\n"
        "ds_read_b64_tr_b16 a[230:231], v14 offset:32768\n"
        "v_mul_f32_e64 v46, v46, |v46|\n"
        "s_waitcnt lgkmcnt(8)\n"
        "v_mfma_f32_16x16x32_bf16 v[104:107], a[240:243], v[168:171], v[104:107]\n"
        "ds_read_b64_tr_b16 a[232:233], v17 offset:36864\n"
        "v_mul_f32_e64 v47, v47, |v47|\n"
        "v_cvt_pk_bf16_f32 v30, v44, v45\n"
        "v_mfma_f32_16x16x32_bf16 v[108:111], a[240:243], v[172:175], v[108:111]\n"
        "ds_read_b64_tr_b16 a[234:235], v13 offset:36864\n"
        "v_cvt_pk_bf16_f32 v31, v46, v47\n"
        "v_mfma_f32_16x16x32_bf16 v[112:115], a[244:247], v[168:171], v[112:115]\n"
        "ds_read_b64_tr_b16 a[236:237], v18 offset:36864\n"
        "v_pk_max_i16 v30, v30, 0\n"
        "v_pk_max_i16 v31, v31, 0\n"
        "v_mfma_f32_16x16x32_bf16 v[116:119], a[244:247], v[172:175], v[116:119]\n"
        "buffer_load_dwordx4 v[68:71], v20, s[36:39], s63 offen sc1\n"
        "ds_read_b64_tr_b16 a[238:239], v14 offset:36864\n"
        "v_or_b32 v30, v248, v30\n"
        "v_or_b32 v31, v249, v31\n"
        "s_waitcnt lgkmcnt(8)\n"
        "v_mfma_f32_16x16x32_bf16 v[104:107], a[248:251], v[176:179], v[104:107]\n"
        "s_add_u32 s49, s62, 0x1000\n"
        "ds_read_b64_tr_b16 a[240:241], v17 offset:40960\n"
        "v_mfma_f32_16x16x32_bf16 v[108:111], a[248:251], v[180:183], v[108:111]\n"
        "s_add_u32 s50, s62, 0xf000\n"
        "buffer_store_dwordx4 v[24:27], v4, s[36:39], s49 offen sc1\n"
        "ds_read_b64_tr_b16 a[242:243], v13 offset:40960\n"
        "v_mfma_f32_16x16x32_bf16 v[112:115], a[252:255], v[176:179], v[112:115]\n"
        "buffer_store_dwordx4 v[28:31], v4, s[36:39], s50 offen sc1\n"
        "ds_read_b64_tr_b16 a[244:245], v18 offset:40960\n"
        "v_mfma_f32_16x16x32_bf16 v[116:119], a[252:255], v[180:183], v[116:119]\n"
        "ds_read_b64_tr_b16 a[246:247], v14 offset:40960\n"
        "s_waitcnt lgkmcnt(8)\n"
        "v_mfma_f32_16x16x32_bf16 v[104:107], a[224:227], v[184:187], v[104:107]\n"
        "ds_read_b64_tr_b16 a[248:249], v17 offset:45056\n"
        "v_mfma_f32_16x16x32_bf16 v[108:111], a[224:227], v[188:191], v[108:111]\n"
        "ds_read_b64_tr_b16 a[250:251], v13 offset:45056\n"
        "v_mfma_f32_16x16x32_bf16 v[112:115], a[228:231], v[184:187], v[112:115]\n"
        "s_waitcnt vmcnt(14)\n"
        "buffer_load_dwordx4 v[120:123], v7, s[36:39], s30 offen sc1\n"
        "ds_read_b64_tr_b16 a[252:253], v18 offset:45056\n"
        "v_mfma_f32_16x16x32_bf16 v[116:119], a[228:231], v[188:191], v[116:119]\n"
        "ds_read_b64_tr_b16 a[254:255], v14 offset:45056\n"
        "s_waitcnt lgkmcnt(8)\n"
        "v_mfma_f32_16x16x32_bf16 v[104:107], a[232:235], v[192:195], v[104:107]\n"
        "buffer_load_dwordx4 v[124:127], v8, s[36:39], s30 offen sc1\n"
        "ds_read_b64_tr_b16 a[224:225], v17 offset:49152\n"
        "v_mfma_f32_16x16x32_bf16 v[108:111], a[232:235], v[196:199], v[108:111]\n"
        "ds_read_b64_tr_b16 a[226:227], v13 offset:49152\n"
        "v_mfma_f32_16x16x32_bf16 v[112:115], a[236:239], v[192:195], v[112:115]\n"
        "buffer_load_dwordx4 v[128:131], v9, s[36:39], s30 offen sc1\n"
        "ds_read_b64_tr_b16 a[228:229], v18 offset:49152\n"
        "v_mfma_f32_16x16x32_bf16 v[116:119], a[236:239], v[196:199], v[116:119]\n"
        "ds_read_b64_tr_b16 a[230:231], v14 offset:49152\n"
        "s_waitcnt lgkmcnt(8)\n"
        "v_mfma_f32_16x16x32_bf16 v[104:107], a[240:243], v[200:203], v[104:107]\n"
        "buffer_load_dwordx4 v[132:135], v10, s[36:39], s30 offen sc1\n"
        "ds_read_b64_tr_b16 a[232:233], v17 offset:53248\n"
        "v_mfma_f32_16x16x32_bf16 v[108:111], a[240:243], v[204:207], v[108:111]\n"
        "ds_read_b64_tr_b16 a[234:235], v13 offset:53248\n"
        "v_mfma_f32_16x16x32_bf16 v[112:115], a[244:247], v[200:203], v[112:115]\n"
        "buffer_load_dwordx4 v[136:139], v7, s[36:39], s31 offen sc1\n"
        "ds_read_b64_tr_b16 a[236:237], v18 offset:53248\n"
        "v_mfma_f32_16x16x32_bf16 v[116:119], a[244:247], v[204:207], v[116:119]\n"
        "ds_read_b64_tr_b16 a[238:239], v14 offset:53248\n"
        "s_waitcnt lgkmcnt(8)\n"
        "v_mfma_f32_16x16x32_bf16 v[104:107], a[248:251], v[208:211], v[104:107]\n"
        "buffer_load_dwordx4 v[140:143], v8, s[36:39], s31 offen sc1\n"
        "ds_read_b64_tr_b16 a[240:241], v17 offset:57344\n"
        "v_mfma_f32_16x16x32_bf16 v[108:111], a[248:251], v[212:215], v[108:111]\n"
        "ds_read_b64_tr_b16 a[242:243], v13 offset:57344\n"
        "v_mfma_f32_16x16x32_bf16 v[112:115], a[252:255], v[208:211], v[112:115]\n"
        "buffer_load_dwordx4 v[144:147], v9, s[36:39], s31 offen sc1\n"
        "ds_read_b64_tr_b16 a[244:245], v18 offset:57344\n"
        "v_mfma_f32_16x16x32_bf16 v[116:119], a[252:255], v[212:215], v[116:119]\n"
        "ds_read_b64_tr_b16 a[246:247], v14 offset:57344\n"
        "s_waitcnt lgkmcnt(8)\n"
        "v_mfma_f32_16x16x32_bf16 v[104:107], a[224:227], v[216:219], v[104:107]\n"
        "ds_read_b64_tr_b16 a[248:249], v17 offset:61440\n"
        "v_mfma_f32_16x16x32_bf16 v[108:111], a[224:227], v[220:223], v[108:111]\n"
        "buffer_load_dwordx4 v[148:151], v10, s[36:39], s31 offen sc1\n"
        "ds_read_b64_tr_b16 a[250:251], v13 offset:61440\n"
        "v_mfma_f32_16x16x32_bf16 v[112:115], a[228:231], v[216:219], v[112:115]\n"
        "ds_read_b64_tr_b16 a[252:253], v18 offset:61440\n"
        "v_mfma_f32_16x16x32_bf16 v[116:119], a[228:231], v[220:223], v[116:119]\n"
        "s_add_u32 s51, s30, 0x3000\n"
        "ds_read_b64_tr_b16 a[254:255], v14 offset:61440\n"
        "s_waitcnt lgkmcnt(8)\n"
        "v_mfma_f32_16x16x32_bf16 v[104:107], a[232:235], v[224:227], v[104:107]\n"
        "v_mfma_f32_16x16x32_bf16 v[108:111], a[232:235], v[228:231], v[108:111]\n"
        "buffer_load_dwordx4 v[152:155], v4, s[36:39], s51 offen sc1\n"
        "v_mfma_f32_16x16x32_bf16 v[112:115], a[236:239], v[224:227], v[112:115]\n"
        "v_mfma_f32_16x16x32_bf16 v[116:119], a[236:239], v[228:231], v[116:119]\n"
        "buffer_load_dwordx4 v[156:159], v4, s[36:39], s51 offen offset:1024 sc1\n"
        "s_waitcnt lgkmcnt(4)\n"
        "v_mfma_f32_16x16x32_bf16 v[104:107], a[240:243], v[232:235], v[104:107]\n"
        "v_mfma_f32_16x16x32_bf16 v[108:111], a[240:243], v[236:239], v[108:111]\n"
        "s_add_u32 s51, s31, 0x3000\n"
        "v_mfma_f32_16x16x32_bf16 v[112:115], a[244:247], v[232:235], v[112:115]\n"
        "v_mfma_f32_16x16x32_bf16 v[116:119], a[244:247], v[236:239], v[116:119]\n"
        "buffer_load_dwordx4 v[160:163], v4, s[36:39], s51 offen sc1\n"
        "s_waitcnt lgkmcnt(0)\n"
        "v_mfma_f32_16x16x32_bf16 v[104:107], a[248:251], v[240:243], v[104:107]\n"
        "v_mfma_f32_16x16x32_bf16 v[108:111], a[248:251], v[244:247], v[108:111]\n"
        "buffer_load_dwordx4 v[164:167], v4, s[36:39], s51 offen offset:1024 sc1\n"
        "v_mfma_f32_16x16x32_bf16 v[112:115], a[252:255], v[240:243], v[112:115]\n"
        "v_mfma_f32_16x16x32_bf16 v[116:119], a[252:255], v[244:247], v[116:119]\n"
        "v_mfma_f32_16x16x32_bf16 v[104:107], v[72:75], v[80:83], v[104:107]\n"
        "v_mfma_f32_16x16x32_bf16 v[108:111], v[72:75], v[84:87], v[108:111]\n"
        "v_mov_b64 v[72:73], v[98:99]\n"
        "s_nop 1\n"
        "v_mfma_f32_16x16x32_bf16 v[112:115], v[72:75], v[80:83], v[112:115]\n"
        "v_mfma_f32_16x16x32_bf16 v[116:119], v[72:75], v[84:87], v[116:119]\n"
        "s_nop 1\n"
        "v_mul_f32_e64 v104, v104, |v104|\n"
        "v_mul_f32_e64 v105, v105, |v105|\n"
        "v_mul_f32_e64 v106, v106, |v106|\n"
        "v_mul_f32_e64 v107, v107, |v107|\n"
        "v_cvt_pk_bf16_f32 v24, v104, v105\n"
        "v_cvt_pk_bf16_f32 v25, v106, v107\n"
        "v_pk_max_i16 v24, v24, 0\n"
        "v_pk_max_i16 v25, v25, 0\n"
        "v_or_b32 v24, v248, v24\n"
        "v_or_b32 v25, v249, v25\n"
        "v_mul_f32_e64 v108, v108, |v108|\n"
        "v_mul_f32_e64 v109, v109, |v109|\n"
        "v_mul_f32_e64 v110, v110, |v110|\n"
        "v_mul_f32_e64 v111, v111, |v111|\n"
        "v_cvt_pk_bf16_f32 v28, v108, v109\n"
        "v_cvt_pk_bf16_f32 v29, v110, v111\n"
        "v_pk_max_i16 v28, v28, 0\n"
        "v_pk_max_i16 v29, v29, 0\n"
        "v_or_b32 v28, v248, v28\n"
        "v_or_b32 v29, v249, v29\n"
        "v_mul_f32_e64 v112, v112, |v112|\n"
        "v_mul_f32_e64 v113, v113, |v113|\n"
        "v_mul_f32_e64 v114, v114, |v114|\n"
        "v_mul_f32_e64 v115, v115, |v115|\n"
        "v_cvt_pk_bf16_f32 v26, v112, v113\n"
        "v_cvt_pk_bf16_f32 v27, v114, v115\n"
        "v_pk_max_i16 v26, v26, 0\n"
        "v_pk_max_i16 v27, v27, 0\n"
        "v_or_b32 v26, v248, v26\n"
        "v_or_b32 v27, v249, v27\n"
        "v_mul_f32_e64 v116, v116, |v116|\n"
        "v_mul_f32_e64 v117, v117, |v117|\n"
        "v_mul_f32_e64 v118, v118, |v118|\n"
        "v_mul_f32_e64 v119, v119, |v119|\n"
        "v_cvt_pk_bf16_f32 v30, v116, v117\n"
        "v_cvt_pk_bf16_f32 v31, v118, v119\n"
        "v_pk_max_i16 v30, v30, 0\n"
        "v_pk_max_i16 v31, v31, 0\n"
        "v_or_b32 v30, v248, v30\n"
        "v_or_b32 v31, v249, v31\n"
        "s_add_u32 s49, s62, 0x2000\n"
        "s_add_u32 s50, s62, 0x10000\n"
        "buffer_store_dwordx4 v[24:27], v4, s[36:39], s49 offen sc1\n"
        "buffer_store_dwordx4 v[28:31], v4, s[36:39], s50 offen sc1\n"
        "s_mov_b32 s59, 0x80008000\n"
        "s_waitcnt vmcnt(10)\n"
        "v_bitop3_b32 v250, v120, v122, v248 bitop3:0x7e\n"
        "v_bitop3_b32 v251, v121, v123, v249 bitop3:0x7e\n"
        "v_or_b32 v252, v250, v251\n"
        "v_bitop3_b32 v250, v124, v126, v248 bitop3:0x7e\n"
        "v_bitop3_b32 v251, v125, v127, v249 bitop3:0x7e\n"
        "v_or3_b32 v252, v252, v250, v251\n"
        "v_bitop3_b32 v250, v128, v130, v248 bitop3:0x7e\n"
        "v_bitop3_b32 v251, v129, v131, v249 bitop3:0x7e\n"
        "v_or3_b32 v252, v252, v250, v251\n"
        "v_bitop3_b32 v250, v132, v134, v248 bitop3:0x7e\n"
        "v_bitop3_b32 v251, v133, v135, v249 bitop3:0x7e\n"
        "v_or3_b32 v252, v252, v250, v251\n"
        "v_and_b32 v252, s59, v252\n"
        "v_cmp_ne_u32 vcc, 0, v252\n"
        "s_nop 0\n"
        "s_cbranch_vccnz .LX30_RETRY_%=\n"
        ".LX30_DONE_%=:\n"
        "v_mfma_f32_16x16x32_bf16 v[32:35], a[0:3], v[120:123], 0\n"
        "buffer_load_dwordx4 v[168:171], v7, s[36:39], s47 offen sc1\n"
        "s_lshl_b32 s50, s46, 7\n"
        "v_lshl_add_u32 v253, v2, 3, s50\n"
        "v_mfma_f32_16x16x32_bf16 v[36:39], a[4:7], v[120:123], 0\n"
        "global_load_dwordx2 v[30:31], v253, s[32:33]\n"
        "v_mfma_f32_16x16x32_bf16 v[40:43], a[8:11], v[120:123], 0\n"
        "s_lshl1_add_u32 s49, s45, s20\n"
        "s_and_b32 s50, s40, 1\n"
        "v_mfma_f32_16x16x32_bf16 v[44:47], a[12:15], v[120:123], 0\n"
        "s_lshl1_add_u32 s50, s49, s50\n"
        "v_mfma_f32_16x16x32_bf16 v[32:35], a[16:19], v[124:127], v[32:35]\n"
        "buffer_load_dwordx4 v[172:175], v8, s[36:39], s47 offen sc1\n"
        "s_lshl3_add_u32 s50, s50, s42\n"
        "v_mfma_f32_16x16x32_bf16 v[36:39], a[20:23], v[124:127], v[36:39]\n"
        "s_lshl_b32 s50, s50, 11\n"
        "v_lshl_add_u32 v253, v3, 12, v253\n"
        "s_waitcnt vmcnt(9)\n"
        "v_bitop3_b32 v250, v136, v138, v248 bitop3:0x7e\n"
        "v_mfma_f32_16x16x32_bf16 v[40:43], a[24:27], v[124:127], v[40:43]\n"
        "v_add_u32 v18, s50, v253\n"
        "v_bitop3_b32 v251, v137, v139, v249 bitop3:0x7e\n"
        "v_or_b32 v252, v250, v251\n"
        "v_mfma_f32_16x16x32_bf16 v[44:47], a[28:31], v[124:127], v[44:47]\n"
        "v_xor_b32 v19, 0x8000, v18\n"
        "v_bitop3_b32 v250, v140, v142, v248 bitop3:0x7e\n"
        "v_mfma_f32_16x16x32_bf16 v[32:35], a[32:35], v[128:131], v[32:35]\n"
        "buffer_load_dwordx4 v[176:179], v9, s[36:39], s47 offen sc1\n"
        "s_lshl2_add_u32 s49, s49, s40\n"
        "s_lshl_b32 s49, s49, 6\n"
        "v_bitop3_b32 v251, v141, v143, v249 bitop3:0x7e\n"
        "v_or3_b32 v252, v252, v250, v251\n"
        "v_mfma_f32_16x16x32_bf16 v[36:39], a[36:39], v[128:131], v[36:39]\n"
        "s_lshl_b32 s50, s46, 14\n"
        "v_bitop3_b32 v250, v144, v146, v248 bitop3:0x7e\n"
        "v_bitop3_b32 v251, v145, v147, v249 bitop3:0x7e\n"
        "v_mfma_f32_16x16x32_bf16 v[40:43], a[40:43], v[128:131], v[40:43]\n"
        "s_add_u32 s49, s49, s50\n"
        "s_add_u32 s49, s49, 0x40000\n"
        "v_or3_b32 v252, v252, v250, v251\n"
        "v_mfma_f32_16x16x32_bf16 v[44:47], a[44:47], v[128:131], v[44:47]\n"
        "v_lshl_add_u32 v11, v3, 4, s49\n"
        "v_bitop3_b32 v250, v148, v150, v248 bitop3:0x7e\n"
        "v_bitop3_b32 v251, v149, v151, v249 bitop3:0x7e\n"
        "v_mfma_f32_16x16x32_bf16 v[32:35], a[48:51], v[132:135], v[32:35]\n"
        "buffer_load_dwordx4 v[180:183], v10, s[36:39], s47 offen sc1\n"
        "v_xor_b32 v21, 0x100, v11\n"
        "v_or3_b32 v252, v252, v250, v251\n"
        "v_and_b32 v252, s59, v252\n"
        "v_mfma_f32_16x16x32_bf16 v[36:39], a[52:55], v[132:135], v[36:39]\n"
        "s_and_b32 s50, s40, 1\n"
        "s_lshl_b32 s50, s50, 2\n"
        "v_cmp_ne_u32 vcc, 0, v252\n"
        "v_mfma_f32_16x16x32_bf16 v[40:43], a[56:59], v[132:135], v[40:43]\n"
        "v_and_b32 v6, 7, v2\n"
        "v_mfma_f32_16x16x32_bf16 v[44:47], a[60:63], v[132:135], v[44:47]\n"
        "v_add_u32 v253, s50, v3\n"
        "s_cbranch_vccnz .LX31_RETRY_%=\n"
        ".LX31_DONE_%=:\n"
        "v_mfma_f32_16x16x32_bf16 v[48:51], a[0:3], v[136:139], 0\n"
        "buffer_load_dwordx4 v[184:187], v7, s[36:39], s48 offen sc1\n"
        "v_xor_b32 v253, v253, v6\n"
        "v_lshlrev_b32 v253, 4, v253\n"
        "v_mfma_f32_16x16x32_bf16 v[52:55], a[4:7], v[136:139], 0\n"
        "v_lshl_add_u32 v253, v6, 8, v253\n"
        "v_mfma_f32_16x16x32_bf16 v[56:59], a[8:11], v[136:139], 0\n"
        "v_lshrrev_b32 v6, 3, v2\n"
        "v_lshl_add_u32 v253, v6, 13, v253\n"
        "s_waitcnt vmcnt(8)\n"
        "v_bitop3_b32 v250, v152, v154, v248 bitop3:0x7e\n"
        "v_mfma_f32_16x16x32_bf16 v[60:63], a[12:15], v[136:139], 0\n"
        "buffer_load_dwordx4 v[188:191], v8, s[36:39], s48 offen sc1\n"
        "s_lshl_b32 s49, s20, 11\n"
        "v_bitop3_b32 v251, v153, v155, v249 bitop3:0x7e\n"
        "v_or_b32 v252, v250, v251\n"
        "v_mfma_f32_16x16x32_bf16 v[48:51], a[16:19], v[140:143], v[48:51]\n"
        "s_add_u32 s49, s49, s42\n"
        "v_bitop3_b32 v250, v156, v158, v248 bitop3:0x7e\n"
        "v_mfma_f32_16x16x32_bf16 v[52:55], a[20:23], v[140:143], v[52:55]\n"
        "s_lshl_b32 s49, s49, 3\n"
        "s_add_u32 s49, s49, 0x20000\n"
        "v_bitop3_b32 v251, v157, v159, v249 bitop3:0x7e\n"
        "v_or3_b32 v252, v252, v250, v251\n"
        "v_mfma_f32_16x16x32_bf16 v[56:59], a[24:27], v[140:143], v[56:59]\n"
        "buffer_load_dwordx4 v[192:195], v9, s[36:39], s48 offen sc1\n"
        "v_add_u32 v5, s49, v253\n"
        "v_bitop3_b32 v250, v160, v162, v248 bitop3:0x7e\n"
        "v_bitop3_b32 v251, v161, v163, v249 bitop3:0x7e\n"
        "v_mfma_f32_16x16x32_bf16 v[60:63], a[28:31], v[140:143], v[60:63]\n"
        "v_xor_b32 v6, 0x4000, v5\n"
        "v_or3_b32 v252, v252, v250, v251\n"
        "v_mfma_f32_16x16x32_bf16 v[48:51], a[32:35], v[144:147], v[48:51]\n"
        "s_lshl_b32 s50, s40, 12\n"
        "v_xor_b32 v253, v1, v3\n"
        "v_bitop3_b32 v250, v164, v166, v248 bitop3:0x7e\n"
        "v_bitop3_b32 v251, v165, v167, v249 bitop3:0x7e\n"
        "v_mfma_f32_16x16x32_bf16 v[52:55], a[36:39], v[144:147], v[52:55]\n"
        "buffer_load_dwordx4 v[196:199], v10, s[36:39], s48 offen sc1\n"
        "v_lshl_add_u32 v13, v253, 4, s50\n"
        "v_or3_b32 v252, v252, v250, v251\n"
        "v_and_b32 v252, s59, v252\n"
        "v_mfma_f32_16x16x32_bf16 v[56:59], a[40:43], v[144:147], v[56:59]\n"
        "v_xor_b32 v14, 64, v13\n"
        "v_xor_b32 v15, 0x80, v13\n"
        "v_cmp_ne_u32 vcc, 0, v252\n"
        "v_mfma_f32_16x16x32_bf16 v[60:63], a[44:47], v[144:147], v[60:63]\n"
        "v_xor_b32 v16, 0xc0, v13\n"
        "v_mfma_f32_16x16x32_bf16 v[48:51], a[48:51], v[148:151], v[48:51]\n"
        "buffer_load_dwordx4 v[96:99], v11, s[36:39], 0 offen sc1\n"
        "s_lshl_b32 s50, s40, 2\n"
        "s_cbranch_vccnz .LX32_RETRY_%=\n"
        ".LX32_DONE_%=:\n"
        "v_swap_b32 v154, v156\n"
        "v_mfma_f32_16x16x32_bf16 v[52:55], a[52:55], v[148:151], v[52:55]\n"
        "v_add_u32 v253, s50, v3\n"
        "v_xor_b32 v253, v253, v2\n"
        "v_swap_b32 v155, v157\n"
        "v_swap_b32 v162, v164\n"
        "v_mfma_f32_16x16x32_bf16 v[56:59], a[56:59], v[148:151], v[56:59]\n"
        "v_lshlrev_b32 v253, 4, v253\n"
        "v_swap_b32 v163, v165\n"
        "v_mfma_f32_16x16x32_bf16 v[60:63], a[60:63], v[148:151], v[60:63]\n"
        "v_lshl_add_u32 v17, v2, 8, v253\n"
        "v_mfma_f32_16x16x32_bf16 v[32:35], a[64:67], v[152:155], v[32:35]\n"
        "s_add_u32 s51, s47, 0x3000\n"
        "v_max3_u32 v252, v65, v67, v69\n"
        "v_max_u32 v252, v252, v71\n"
        "v_mfma_f32_16x16x32_bf16 v[36:39], a[68:71], v[152:155], v[36:39]\n"
        "v_cmp_le_u32 vcc, s1, v252\n"
        "v_mfma_f32_16x16x32_bf16 v[40:43], a[72:75], v[152:155], v[40:43]\n"
        "buffer_load_dwordx4 v[200:203], v4, s[36:39], s51 offen sc1\n"
        "v_mfma_f32_16x16x32_bf16 v[44:47], a[76:79], v[152:155], v[44:47]\n"
        "v_mfma_f32_16x16x32_bf16 v[48:51], a[64:67], v[156:159], v[48:51]\n"
        "buffer_load_dwordx4 v[204:207], v4, s[36:39], s51 offen offset:1024 sc1\n"
        "s_cbranch_vccnz .LXR_RETRY_%=\n"
        ".LXR_DONE_%=:\n"
        "v_mfma_f32_16x16x32_bf16 v[52:55], a[68:71], v[156:159], v[52:55]\n"
        "ds_write_b128 v12, v[64:67]\n"
        "s_waitcnt vmcnt(7)\n"
        "v_bitop3_b32 v250, v168, v170, v248 bitop3:0x7e\n"
        "v_mfma_f32_16x16x32_bf16 v[56:59], a[72:75], v[156:159], v[56:59]\n"
        "s_add_u32 s51, s48, 0x3000\n"
        "v_xor_b32 v12, 64, v12\n"
        "v_bitop3_b32 v251, v169, v171, v249 bitop3:0x7e\n"
        "v_or_b32 v252, v250, v251\n"
        "s_nop 0\n"
        "s_cbranch_execz .LWARM_RET_%=\n"
        "v_mfma_f32_16x16x32_bf16 v[60:63], a[76:79], v[156:159], v[60:63]\n"
        "ds_write_b128 v12, v[68:71] offset:1024\n"
        "v_bitop3_b32 v250, v172, v174, v248 bitop3:0x7e\n"
        "v_mfma_f32_16x16x32_bf16 v[32:35], a[80:83], v[160:163], v[32:35]\n"
        "v_bitop3_b32 v251, v173, v175, v249 bitop3:0x7e\n"
        "v_or3_b32 v252, v252, v250, v251\n"
        "v_mfma_f32_16x16x32_bf16 v[36:39], a[84:87], v[160:163], v[36:39]\n"
        "buffer_load_dwordx4 v[208:211], v4, s[36:39], s51 offen sc1\n"
        "v_bitop3_b32 v250, v176, v178, v248 bitop3:0x7e\n"
        "v_bitop3_b32 v251, v177, v179, v249 bitop3:0x7e\n"
        "v_mfma_f32_16x16x32_bf16 v[40:43], a[88:91], v[160:163], v[40:43]\n"
        "v_or3_b32 v252, v252, v250, v251\n"
        "v_mfma_f32_16x16x32_bf16 v[44:47], a[92:95], v[160:163], v[44:47]\n"
        "buffer_load_dwordx4 v[212:215], v4, s[36:39], s51 offen offset:1024 sc1\n"
        "v_bitop3_b32 v250, v180, v182, v248 bitop3:0x7e\n"
        "v_bitop3_b32 v251, v181, v183, v249 bitop3:0x7e\n"
        "v_mfma_f32_16x16x32_bf16 v[48:51], a[80:83], v[164:167], v[48:51]\n"
        "v_or3_b32 v252, v252, v250, v251\n"
        "v_and_b32 v252, s59, v252\n"
        "v_mfma_f32_16x16x32_bf16 v[52:55], a[84:87], v[164:167], v[52:55]\n"
        "buffer_load_dwordx4 v[24:27], v21, s[36:39], 0 offen sc1\n"
        "v_cmp_ne_u32 vcc, 0, v252\n"
        "v_mfma_f32_16x16x32_bf16 v[56:59], a[88:91], v[164:167], v[56:59]\n"
        "v_mfma_f32_16x16x32_bf16 v[60:63], a[92:95], v[164:167], v[60:63]\n"
        "s_cbranch_vccnz .LX33_RETRY_%=\n"
        ".LX33_DONE_%=:\n"
        "v_mfma_f32_16x16x32_bf16 v[64:67], a[0:3], v[168:171], 0\n"
        "s_add_u32 s51, s30, 0x1000\n"
        "s_waitcnt lgkmcnt(0)\n"
        "s_barrier\n"
        "v_mfma_f32_16x16x32_bf16 v[68:71], a[4:7], v[168:171], 0\n"
        "ds_read_b64 v[22:23], v5\n"
        "v_mfma_f32_16x16x32_bf16 v[72:75], a[8:11], v[168:171], 0\n"
        "v_mfma_f32_16x16x32_bf16 v[76:79], a[12:15], v[168:171], 0\n"
        "buffer_load_dwordx4 v[216:219], v7, s[36:39], s51 offen sc1\n"
        "ds_read_b64 v[28:29], v5 offset:128\n"
        "v_mfma_f32_16x16x32_bf16 v[64:67], a[16:19], v[172:175], v[64:67]\n"
        "ds_read_b64 v[100:101], v6\n"
        "v_mfma_f32_16x16x32_bf16 v[68:71], a[20:23], v[172:175], v[68:71]\n"
        "s_waitcnt vmcnt(7)\n"
        "v_bitop3_b32 v250, v184, v186, v248 bitop3:0x7e\n"
        "v_mfma_f32_16x16x32_bf16 v[72:75], a[24:27], v[172:175], v[72:75]\n"
        "buffer_load_dwordx4 v[220:223], v8, s[36:39], s51 offen sc1\n"
        "ds_read_b64 v[254:255], v6 offset:128\n"
        "v_bitop3_b32 v251, v185, v187, v249 bitop3:0x7e\n"
        "v_or_b32 v252, v250, v251\n"
        "v_mfma_f32_16x16x32_bf16 v[76:79], a[28:31], v[172:175], v[76:79]\n"
        "v_bitop3_b32 v250, v188, v190, v248 bitop3:0x7e\n"
        "v_mfma_f32_16x16x32_bf16 v[64:67], a[32:35], v[176:179], v[64:67]\n"
        "v_lshlrev_b32 v20, 16, v30\n"
        "v_bitop3_b32 v251, v189, v191, v249 bitop3:0x7e\n"
        "v_or3_b32 v252, v252, v250, v251\n"
        "v_mfma_f32_16x16x32_bf16 v[68:71], a[36:39], v[176:179], v[68:71]\n"
        "buffer_load_dwordx4 v[224:227], v9, s[36:39], s51 offen sc1\n"
        "v_and_b32 v12, s1, v30\n"
        "v_bitop3_b32 v250, v192, v194, v248 bitop3:0x7e\n"
        "v_bitop3_b32 v251, v193, v195, v249 bitop3:0x7e\n"
        "v_mfma_f32_16x16x32_bf16 v[72:75], a[40:43], v[176:179], v[72:75]\n"
        "v_or3_b32 v252, v252, v250, v251\n"
        "v_mfma_f32_16x16x32_bf16 v[76:79], a[44:47], v[176:179], v[76:79]\n"
        "s_waitcnt lgkmcnt(3)\n"
        "v_lshlrev_b32 v104, 16, v22\n"
        "v_bitop3_b32 v250, v196, v198, v248 bitop3:0x7e\n"
        "v_bitop3_b32 v251, v197, v199, v249 bitop3:0x7e\n"
        "v_mfma_f32_16x16x32_bf16 v[64:67], a[48:51], v[180:183], v[64:67]\n"
        "buffer_load_dwordx4 v[228:231], v10, s[36:39], s51 offen sc1\n"
        "v_and_b32 v105, s1, v22\n"
        "v_or3_b32 v252, v252, v250, v251\n"
        "v_and_b32 v252, s59, v252\n"
        "v_mfma_f32_16x16x32_bf16 v[68:71], a[52:55], v[180:183], v[68:71]\n"
        "v_cmp_ne_u32 vcc, 0, v252\n"
        "v_mfma_f32_16x16x32_bf16 v[72:75], a[56:59], v[180:183], v[72:75]\n"
        "v_add_f32 v104, v104, v20\n"
        "v_mfma_f32_16x16x32_bf16 v[76:79], a[60:63], v[180:183], v[76:79]\n"
        "s_cbranch_vccnz .LX34_RETRY_%=\n"
        ".LX34_DONE_%=:\n"
        "v_mfma_f32_16x16x32_bf16 v[80:83], a[0:3], v[184:187], 0\n"
        "s_add_u32 s51, s30, 0x2000\n"
        "v_add_f32 v105, v105, v12\n"
        "v_mfma_f32_16x16x32_bf16 v[84:87], a[4:7], v[184:187], 0\n"
        "v_lshlrev_b32 v108, 16, v23\n"
        "v_mfma_f32_16x16x32_bf16 v[88:91], a[8:11], v[184:187], 0\n"
        "v_and_b32 v109, s1, v23\n"
        "s_waitcnt vmcnt(5)\n"
        "v_bitop3_b32 v250, v200, v202, v248 bitop3:0x7e\n"
        "v_mfma_f32_16x16x32_bf16 v[92:95], a[12:15], v[184:187], 0\n"
        "buffer_load_dwordx4 v[232:235], v7, s[36:39], s51 offen sc1\n"
        "v_bitop3_b32 v251, v201, v203, v249 bitop3:0x7e\n"
        "v_or_b32 v252, v250, v251\n"
        "v_mfma_f32_16x16x32_bf16 v[80:83], a[16:19], v[188:191], v[80:83]\n"
        "v_add_f32 v108, v108, v20\n"
        "v_bitop3_b32 v250, v204, v206, v248 bitop3:0x7e\n"
        "v_mfma_f32_16x16x32_bf16 v[84:87], a[20:23], v[188:191], v[84:87]\n"
        "v_add_f32 v109, v109, v12\n"
        "v_bitop3_b32 v251, v205, v207, v249 bitop3:0x7e\n"
        "v_or3_b32 v252, v252, v250, v251\n"
        "v_mfma_f32_16x16x32_bf16 v[88:91], a[24:27], v[188:191], v[88:91]\n"
        "buffer_load_dwordx4 v[236:239], v8, s[36:39], s51 offen sc1\n"
        "v_bitop3_b32 v250, v208, v210, v248 bitop3:0x7e\n"
        "v_bitop3_b32 v251, v209, v211, v249 bitop3:0x7e\n"
        "v_mfma_f32_16x16x32_bf16 v[92:95], a[28:31], v[188:191], v[92:95]\n"
        "s_waitcnt lgkmcnt(1)\n"
        "v_lshlrev_b32 v112, 16, v100\n"
        "v_or3_b32 v252, v252, v250, v251\n"
        "v_mfma_f32_16x16x32_bf16 v[80:83], a[32:35], v[192:195], v[80:83]\n"
        "v_and_b32 v113, s1, v100\n"
        "v_bitop3_b32 v250, v212, v214, v248 bitop3:0x7e\n"
        "v_bitop3_b32 v251, v213, v215, v249 bitop3:0x7e\n"
        "v_mfma_f32_16x16x32_bf16 v[84:87], a[36:39], v[192:195], v[84:87]\n"
        "buffer_load_dwordx4 v[240:243], v9, s[36:39], s51 offen sc1\n"
        "v_or3_b32 v252, v252, v250, v251\n"
        "v_and_b32 v252, s59, v252\n"
        "v_mfma_f32_16x16x32_bf16 v[88:91], a[40:43], v[192:195], v[88:91]\n"
        "v_add_f32 v112, v112, v20\n"
        "v_cmp_ne_u32 vcc, 0, v252\n"
        "v_mfma_f32_16x16x32_bf16 v[92:95], a[44:47], v[192:195], v[92:95]\n"
        "v_add_f32 v113, v113, v12\n"
        "v_mfma_f32_16x16x32_bf16 v[80:83], a[48:51], v[196:199], v[80:83]\n"
        "buffer_load_dwordx4 v[244:247], v10, s[36:39], s51 offen sc1\n"
        "s_cbranch_vccnz .LX35_RETRY_%=\n"
        ".LX35_DONE_%=:\n"
        "v_swap_b32 v202, v204\n"
        "v_mfma_f32_16x16x32_bf16 v[84:87], a[52:55], v[196:199], v[84:87]\n"
        "v_lshlrev_b32 v116, 16, v101\n"
        "v_swap_b32 v203, v205\n"
        "v_swap_b32 v210, v212\n"
        "v_mfma_f32_16x16x32_bf16 v[88:91], a[56:59], v[196:199], v[88:91]\n"
        "v_and_b32 v117, s1, v101\n"
        "v_swap_b32 v211, v213\n"
        "v_mfma_f32_16x16x32_bf16 v[92:95], a[60:63], v[196:199], v[92:95]\n"
        "v_mfma_f32_16x16x32_bf16 v[64:67], a[64:67], v[200:203], v[64:67]\n"
        "s_add_u32 s51, s31, 0x1000\n"
        "v_add_f32 v116, v116, v20\n"
        "v_mfma_f32_16x16x32_bf16 v[68:71], a[68:71], v[200:203], v[68:71]\n"
        "v_add_f32 v117, v117, v12\n"
        "v_mfma_f32_16x16x32_bf16 v[72:75], a[72:75], v[200:203], v[72:75]\n"
        "v_mfma_f32_16x16x32_bf16 v[76:79], a[76:79], v[200:203], v[76:79]\n"
        "buffer_load_dwordx4 v[120:123], v7, s[36:39], s51 offen sc1\n"
        "v_lshlrev_b32 v20, 16, v31\n"
        "v_mfma_f32_16x16x32_bf16 v[80:83], a[64:67], v[204:207], v[80:83]\n"
        "v_and_b32 v12, s1, v31\n"
        "v_mfma_f32_16x16x32_bf16 v[84:87], a[68:71], v[204:207], v[84:87]\n"
        "s_waitcnt vmcnt(5)\n"
        "v_bitop3_b32 v250, v216, v218, v248 bitop3:0x7e\n"
        "v_mfma_f32_16x16x32_bf16 v[88:91], a[72:75], v[204:207], v[88:91]\n"
        "buffer_load_dwordx4 v[124:127], v8, s[36:39], s51 offen sc1\n"
        "v_lshlrev_b32 v106, 16, v28\n"
        "v_bitop3_b32 v251, v217, v219, v249 bitop3:0x7e\n"
        "v_or_b32 v252, v250, v251\n"
        "v_mfma_f32_16x16x32_bf16 v[92:95], a[76:79], v[204:207], v[92:95]\n"
        "v_bitop3_b32 v250, v220, v222, v248 bitop3:0x7e\n"
        "v_mfma_f32_16x16x32_bf16 v[64:67], a[80:83], v[208:211], v[64:67]\n"
        "v_and_b32 v107, s1, v28\n"
        "v_bitop3_b32 v251, v221, v223, v249 bitop3:0x7e\n"
        "v_or3_b32 v252, v252, v250, v251\n"
        "v_mfma_f32_16x16x32_bf16 v[68:71], a[84:87], v[208:211], v[68:71]\n"
        "buffer_load_dwordx4 v[128:131], v9, s[36:39], s51 offen sc1\n"
        "v_add_f32 v106, v106, v20\n"
        "v_bitop3_b32 v250, v224, v226, v248 bitop3:0x7e\n"
        "v_bitop3_b32 v251, v225, v227, v249 bitop3:0x7e\n"
        "v_mfma_f32_16x16x32_bf16 v[72:75], a[88:91], v[208:211], v[72:75]\n"
        "v_or3_b32 v252, v252, v250, v251\n"
        "v_mfma_f32_16x16x32_bf16 v[76:79], a[92:95], v[208:211], v[76:79]\n"
        "v_add_f32 v107, v107, v12\n"
        "v_bitop3_b32 v250, v228, v230, v248 bitop3:0x7e\n"
        "v_bitop3_b32 v251, v229, v231, v249 bitop3:0x7e\n"
        "v_mfma_f32_16x16x32_bf16 v[80:83], a[80:83], v[212:215], v[80:83]\n"
        "buffer_load_dwordx4 v[132:135], v10, s[36:39], s51 offen sc1\n"
        "v_lshlrev_b32 v110, 16, v29\n"
        "v_or3_b32 v252, v252, v250, v251\n"
        "v_and_b32 v252, s59, v252\n"
        "v_mfma_f32_16x16x32_bf16 v[84:87], a[84:87], v[212:215], v[84:87]\n"
        "v_cmp_ne_u32 vcc, 0, v252\n"
        "v_mfma_f32_16x16x32_bf16 v[88:91], a[88:91], v[212:215], v[88:91]\n"
        "v_and_b32 v111, s1, v29\n"
        "v_mfma_f32_16x16x32_bf16 v[92:95], a[92:95], v[212:215], v[92:95]\n"
        "s_cbranch_vccnz .LX36_RETRY_%=\n"
        ".LX36_DONE_%=:\n"
        "v_mfma_f32_16x16x32_bf16 v[32:35], a[96:99], v[216:219], v[32:35]\n"
        "s_add_u32 s51, s31, 0x2000\n"
        "v_add_f32 v110, v110, v20\n"
        "v_mfma_f32_16x16x32_bf16 v[36:39], a[100:103], v[216:219], v[36:39]\n"
        "v_add_f32 v111, v111, v12\n"
        "v_mfma_f32_16x16x32_bf16 v[40:43], a[104:107], v[216:219], v[40:43]\n"
        "v_mfma_f32_16x16x32_bf16 v[44:47], a[108:111], v[216:219], v[44:47]\n"
        "buffer_load_dwordx4 v[136:139], v7, s[36:39], s51 offen sc1\n"
        "s_waitcnt lgkmcnt(0)\n"
        "v_lshlrev_b32 v114, 16, v254\n"
        "v_mfma_f32_16x16x32_bf16 v[32:35], a[112:115], v[220:223], v[32:35]\n"
        "v_and_b32 v115, s1, v254\n"
        "v_mfma_f32_16x16x32_bf16 v[36:39], a[116:119], v[220:223], v[36:39]\n"
        "s_waitcnt vmcnt(5)\n"
        "v_bitop3_b32 v250, v232, v234, v248 bitop3:0x7e\n"
        "v_mfma_f32_16x16x32_bf16 v[40:43], a[120:123], v[220:223], v[40:43]\n"
        "buffer_load_dwordx4 v[140:143], v8, s[36:39], s51 offen sc1\n"
        "v_add_f32 v114, v114, v20\n"
        "v_bitop3_b32 v251, v233, v235, v249 bitop3:0x7e\n"
        "v_or_b32 v252, v250, v251\n"
        "v_mfma_f32_16x16x32_bf16 v[44:47], a[124:127], v[220:223], v[44:47]\n"
        "v_bitop3_b32 v250, v236, v238, v248 bitop3:0x7e\n"
        "v_mfma_f32_16x16x32_bf16 v[32:35], a[128:131], v[224:227], v[32:35]\n"
        "v_add_f32 v115, v115, v12\n"
        "v_bitop3_b32 v251, v237, v239, v249 bitop3:0x7e\n"
        "v_or3_b32 v252, v252, v250, v251\n"
        "v_mfma_f32_16x16x32_bf16 v[36:39], a[132:135], v[224:227], v[36:39]\n"
        "buffer_load_dwordx4 v[144:147], v9, s[36:39], s51 offen sc1\n"
        "v_lshlrev_b32 v118, 16, v255\n"
        "v_bitop3_b32 v250, v240, v242, v248 bitop3:0x7e\n"
        "v_bitop3_b32 v251, v241, v243, v249 bitop3:0x7e\n"
        "v_mfma_f32_16x16x32_bf16 v[40:43], a[136:139], v[224:227], v[40:43]\n"
        "v_or3_b32 v252, v252, v250, v251\n"
        "v_mfma_f32_16x16x32_bf16 v[44:47], a[140:143], v[224:227], v[44:47]\n"
        "v_and_b32 v119, s1, v255\n"
        "v_bitop3_b32 v250, v244, v246, v248 bitop3:0x7e\n"
        "v_bitop3_b32 v251, v245, v247, v249 bitop3:0x7e\n"
        "v_mfma_f32_16x16x32_bf16 v[32:35], a[144:147], v[228:231], v[32:35]\n"
        "buffer_load_dwordx4 v[148:151], v10, s[36:39], s51 offen sc1\n"
        "v_add_f32 v118, v118, v20\n"
        "v_or3_b32 v252, v252, v250, v251\n"
        "v_and_b32 v252, s59, v252\n"
        "v_mfma_f32_16x16x32_bf16 v[36:39], a[148:151], v[228:231], v[36:39]\n"
        "v_cmp_ne_u32 vcc, 0, v252\n"
        "v_mfma_f32_16x16x32_bf16 v[40:43], a[152:155], v[228:231], v[40:43]\n"
        "v_add_f32 v119, v119, v12\n"
        "v_mfma_f32_16x16x32_bf16 v[44:47], a[156:159], v[228:231], v[44:47]\n"
        "s_cbranch_vccnz .LX37_RETRY_%=\n"
        ".LX37_DONE_%=:\n"
        "v_mfma_f32_16x16x32_bf16 v[32:35], a[160:163], v[232:235], v[32:35]\n"
        "s_add_u32 s51, s47, 0x1000\n"
        "v_bitop3_b32 v250, v97, v99, s41 bitop3:0x7e\n"
        "v_bitop3_b32 v251, v25, v27, s41 bitop3:0x7e\n"
        "v_mfma_f32_16x16x32_bf16 v[36:39], a[164:167], v[232:235], v[36:39]\n"
        "v_or_b32 v252, v250, v251\n"
        "v_cmp_ne_u32 vcc, 0, v252\n"
        "v_mfma_f32_16x16x32_bf16 v[40:43], a[168:171], v[232:235], v[40:43]\n"
        "v_mfma_f32_16x16x32_bf16 v[44:47], a[172:175], v[232:235], v[44:47]\n"
        "buffer_load_dwordx4 v[152:155], v7, s[36:39], s51 offen sc1\n"
        "v_mfma_f32_16x16x32_bf16 v[32:35], a[176:179], v[236:239], v[32:35]\n"
        "s_cbranch_vccnz .LXS_RETRY_%=\n"
        ".LXS_DONE_%=:\n"
        "v_mfma_f32_16x16x32_bf16 v[36:39], a[180:183], v[236:239], v[36:39]\n"
        "s_waitcnt vmcnt(5)\n"
        "v_bitop3_b32 v250, v120, v122, v248 bitop3:0x7e\n"
        "v_mfma_f32_16x16x32_bf16 v[40:43], a[184:187], v[236:239], v[40:43]\n"
        "buffer_load_dwordx4 v[156:159], v8, s[36:39], s51 offen sc1\n"
        "v_bitop3_b32 v251, v121, v123, v249 bitop3:0x7e\n"
        "v_or_b32 v252, v250, v251\n"
        "v_mfma_f32_16x16x32_bf16 v[44:47], a[188:191], v[236:239], v[44:47]\n"
        "v_bitop3_b32 v250, v124, v126, v248 bitop3:0x7e\n"
        "v_mfma_f32_16x16x32_bf16 v[32:35], a[192:195], v[240:243], v[32:35]\n"
        "v_bitop3_b32 v251, v125, v127, v249 bitop3:0x7e\n"
        "v_or3_b32 v252, v252, v250, v251\n"
        "v_mfma_f32_16x16x32_bf16 v[36:39], a[196:199], v[240:243], v[36:39]\n"
        "buffer_load_dwordx4 v[160:163], v9, s[36:39], s51 offen sc1\n"
        "v_bitop3_b32 v250, v128, v130, v248 bitop3:0x7e\n"
        "v_bitop3_b32 v251, v129, v131, v249 bitop3:0x7e\n"
        "v_mfma_f32_16x16x32_bf16 v[40:43], a[200:203], v[240:243], v[40:43]\n"
        "v_or3_b32 v252, v252, v250, v251\n"
        "v_mfma_f32_16x16x32_bf16 v[44:47], a[204:207], v[240:243], v[44:47]\n"
        "v_bitop3_b32 v250, v132, v134, v248 bitop3:0x7e\n"
        "v_bitop3_b32 v251, v133, v135, v249 bitop3:0x7e\n"
        "v_mfma_f32_16x16x32_bf16 v[32:35], a[208:211], v[244:247], v[32:35]\n"
        "buffer_load_dwordx4 v[164:167], v10, s[36:39], s51 offen sc1\n"
        "v_or3_b32 v252, v252, v250, v251\n"
        "v_and_b32 v252, s59, v252\n"
        "v_mfma_f32_16x16x32_bf16 v[36:39], a[212:215], v[244:247], v[36:39]\n"
        "v_cmp_ne_u32 vcc, 0, v252\n"
        "v_mfma_f32_16x16x32_bf16 v[40:43], a[216:219], v[244:247], v[40:43]\n"
        "v_mfma_f32_16x16x32_bf16 v[44:47], a[220:223], v[244:247], v[44:47]\n"
        "s_cbranch_vccnz .LX38_RETRY_%=\n"
        ".LX38_DONE_%=:\n"
        "v_mfma_f32_16x16x32_bf16 v[48:51], a[96:99], v[120:123], v[48:51]\n"
        "s_add_u32 s51, s47, 0x2000\n"
        "v_mfma_f32_16x16x32_bf16 v[52:55], a[100:103], v[120:123], v[52:55]\n"
        "v_mfma_f32_16x16x32_bf16 v[56:59], a[104:107], v[120:123], v[56:59]\n"
        "v_mfma_f32_16x16x32_bf16 v[60:63], a[108:111], v[120:123], v[60:63]\n"
        "buffer_load_dwordx4 v[168:171], v7, s[36:39], s51 offen sc1\n"
        "ds_write_b128 v13, v[32:35]\n"
        "v_mfma_f32_16x16x32_bf16 v[48:51], a[112:115], v[124:127], v[48:51]\n"
        "ds_write_b128 v14, v[36:39] offset:1024\n"
        "v_mfma_f32_16x16x32_bf16 v[52:55], a[116:119], v[124:127], v[52:55]\n"
        "s_waitcnt vmcnt(5)\n"
        "v_bitop3_b32 v250, v136, v138, v248 bitop3:0x7e\n"
        "v_mfma_f32_16x16x32_bf16 v[56:59], a[120:123], v[124:127], v[56:59]\n"
        "buffer_load_dwordx4 v[172:175], v8, s[36:39], s51 offen sc1\n"
        "ds_write_b128 v15, v[40:43] offset:2048\n"
        "v_bitop3_b32 v251, v137, v139, v249 bitop3:0x7e\n"
        "v_or_b32 v252, v250, v251\n"
        "v_mfma_f32_16x16x32_bf16 v[60:63], a[124:127], v[124:127], v[60:63]\n"
        "v_bitop3_b32 v250, v140, v142, v248 bitop3:0x7e\n"
        "v_mfma_f32_16x16x32_bf16 v[48:51], a[128:131], v[128:131], v[48:51]\n"
        "ds_write_b128 v16, v[44:47] offset:3072\n"
        "v_bitop3_b32 v251, v141, v143, v249 bitop3:0x7e\n"
        "v_or3_b32 v252, v252, v250, v251\n"
        "v_mfma_f32_16x16x32_bf16 v[52:55], a[132:135], v[128:131], v[52:55]\n"
        "buffer_load_dwordx4 v[176:179], v9, s[36:39], s51 offen sc1\n"
        "v_bitop3_b32 v250, v144, v146, v248 bitop3:0x7e\n"
        "v_bitop3_b32 v251, v145, v147, v249 bitop3:0x7e\n"
        "v_mfma_f32_16x16x32_bf16 v[56:59], a[136:139], v[128:131], v[56:59]\n"
        "v_or3_b32 v252, v252, v250, v251\n"
        "v_mfma_f32_16x16x32_bf16 v[60:63], a[140:143], v[128:131], v[60:63]\n"
        "v_bitop3_b32 v250, v148, v150, v248 bitop3:0x7e\n"
        "v_bitop3_b32 v251, v149, v151, v249 bitop3:0x7e\n"
        "v_mfma_f32_16x16x32_bf16 v[48:51], a[144:147], v[132:135], v[48:51]\n"
        "buffer_load_dwordx4 v[180:183], v10, s[36:39], s51 offen sc1\n"
        "v_or3_b32 v252, v252, v250, v251\n"
        "v_and_b32 v252, s59, v252\n"
        "v_mfma_f32_16x16x32_bf16 v[52:55], a[148:151], v[132:135], v[52:55]\n"
        "v_cmp_ne_u32 vcc, 0, v252\n"
        "v_mfma_f32_16x16x32_bf16 v[56:59], a[152:155], v[132:135], v[56:59]\n"
        "s_waitcnt lgkmcnt(0)\n"
        "s_barrier\n"
        "v_mfma_f32_16x16x32_bf16 v[60:63], a[156:159], v[132:135], v[60:63]\n"
        "ds_read_b128 v[32:35], v17\n"
        "ds_read_b128 v[36:39], v17 offset:4096\n"
        "s_cbranch_vccnz .LX39_RETRY_%=\n"
        ".LX39_DONE_%=:\n"
        "v_mfma_f32_16x16x32_bf16 v[48:51], a[160:163], v[136:139], v[48:51]\n"
        "s_add_u32 s51, s48, 0x1000\n"
        "ds_read_b128 v[40:43], v17 offset:8192\n"
        "v_mfma_f32_16x16x32_bf16 v[52:55], a[164:167], v[136:139], v[52:55]\n"
        "ds_read_b128 v[44:47], v17 offset:12288\n"
        "v_mfma_f32_16x16x32_bf16 v[56:59], a[168:171], v[136:139], v[56:59]\n"
        "v_mfma_f32_16x16x32_bf16 v[60:63], a[172:175], v[136:139], v[60:63]\n"
        "buffer_load_dwordx4 v[184:187], v7, s[36:39], s51 offen sc1\n"
        "v_mfma_f32_16x16x32_bf16 v[48:51], a[176:179], v[140:143], v[48:51]\n"
        "v_mfma_f32_16x16x32_bf16 v[52:55], a[180:183], v[140:143], v[52:55]\n"
        "s_waitcnt lgkmcnt(0)\n"
        "v_add_f32 v32, v32, v36\n"
        "s_waitcnt vmcnt(5)\n"
        "v_bitop3_b32 v250, v152, v154, v248 bitop3:0x7e\n"
        "v_mfma_f32_16x16x32_bf16 v[56:59], a[184:187], v[140:143], v[56:59]\n"
        "buffer_load_dwordx4 v[188:191], v8, s[36:39], s51 offen sc1\n"
        "v_add_f32 v40, v40, v44\n"
        "v_add_f32 v32, v32, v40\n"
        "v_bitop3_b32 v251, v153, v155, v249 bitop3:0x7e\n"
        "v_or_b32 v252, v250, v251\n"
        "v_mfma_f32_16x16x32_bf16 v[60:63], a[188:191], v[140:143], v[60:63]\n"
        "v_fma_f32 v32, v32, v96, v104\n"
        "v_add_f32 v33, v33, v37\n"
        "v_bitop3_b32 v250, v156, v158, v248 bitop3:0x7e\n"
        "v_mfma_f32_16x16x32_bf16 v[48:51], a[192:195], v[144:147], v[48:51]\n"
        "v_add_f32 v41, v41, v45\n"
        "v_bitop3_b32 v251, v157, v159, v249 bitop3:0x7e\n"
        "v_or3_b32 v252, v252, v250, v251\n"
        "v_mfma_f32_16x16x32_bf16 v[52:55], a[196:199], v[144:147], v[52:55]\n"
        "buffer_load_dwordx4 v[192:195], v9, s[36:39], s51 offen sc1\n"
        "v_add_f32 v33, v33, v41\n"
        "v_fma_f32 v33, v33, v96, v105\n"
        "v_bitop3_b32 v250, v160, v162, v248 bitop3:0x7e\n"
        "v_bitop3_b32 v251, v161, v163, v249 bitop3:0x7e\n"
        "v_mfma_f32_16x16x32_bf16 v[56:59], a[200:203], v[144:147], v[56:59]\n"
        "v_add_f32 v34, v34, v38\n"
        "v_add_f32 v42, v42, v46\n"
        "v_or3_b32 v252, v252, v250, v251\n"
        "v_mfma_f32_16x16x32_bf16 v[60:63], a[204:207], v[144:147], v[60:63]\n"
        "v_add_f32 v34, v34, v42\n"
        "v_fma_f32 v34, v34, v96, v106\n"
        "v_bitop3_b32 v250, v164, v166, v248 bitop3:0x7e\n"
        "v_bitop3_b32 v251, v165, v167, v249 bitop3:0x7e\n"
        "v_mfma_f32_16x16x32_bf16 v[48:51], a[208:211], v[148:151], v[48:51]\n"
        "buffer_load_dwordx4 v[196:199], v10, s[36:39], s51 offen sc1\n"
        "v_add_f32 v35, v35, v39\n"
        "v_or3_b32 v252, v252, v250, v251\n"
        "v_and_b32 v252, s59, v252\n"
        "v_mfma_f32_16x16x32_bf16 v[52:55], a[212:215], v[148:151], v[52:55]\n"
        "v_add_f32 v43, v43, v47\n"
        "v_add_f32 v35, v35, v43\n"
        "v_cmp_ne_u32 vcc, 0, v252\n"
        "v_mfma_f32_16x16x32_bf16 v[56:59], a[216:219], v[148:151], v[56:59]\n"
        "v_fma_f32 v35, v35, v96, v107\n"
        "v_cvt_pk_bf16_f32 v36, v32, v33\n"
        "v_mfma_f32_16x16x32_bf16 v[60:63], a[220:223], v[148:151], v[60:63]\n"
        "v_cvt_pk_bf16_f32 v37, v34, v35\n"
        "s_cbranch_vccnz .LX310_RETRY_%=\n"
        ".LX310_DONE_%=:\n"
        "v_mfma_f32_16x16x32_bf16 v[64:67], a[96:99], v[152:155], v[64:67]\n"
        "s_add_u32 s51, s48, 0x2000\n"
        "v_mfma_f32_16x16x32_bf16 v[68:71], a[100:103], v[152:155], v[68:71]\n"
        "v_mfma_f32_16x16x32_bf16 v[72:75], a[104:107], v[152:155], v[72:75]\n"
        "v_mfma_f32_16x16x32_bf16 v[76:79], a[108:111], v[152:155], v[76:79]\n"
        "buffer_load_dwordx4 v[200:203], v7, s[36:39], s51 offen sc1\n"
        "ds_write_b128 v13, v[48:51] offset:16384\n"
        "v_mfma_f32_16x16x32_bf16 v[64:67], a[112:115], v[156:159], v[64:67]\n"
        "ds_write_b128 v14, v[52:55] offset:17408\n"
        "v_mfma_f32_16x16x32_bf16 v[68:71], a[116:119], v[156:159], v[68:71]\n"
        "s_waitcnt vmcnt(5)\n"
        "v_bitop3_b32 v250, v168, v170, v248 bitop3:0x7e\n"
        "v_mfma_f32_16x16x32_bf16 v[72:75], a[120:123], v[156:159], v[72:75]\n"
        "buffer_load_dwordx4 v[204:207], v8, s[36:39], s51 offen sc1\n"
        "ds_write_b128 v15, v[56:59] offset:18432\n"
        "v_bitop3_b32 v251, v169, v171, v249 bitop3:0x7e\n"
        "v_or_b32 v252, v250, v251\n"
        "v_mfma_f32_16x16x32_bf16 v[76:79], a[124:127], v[156:159], v[76:79]\n"
        "v_bitop3_b32 v250, v172, v174, v248 bitop3:0x7e\n"
        "v_mfma_f32_16x16x32_bf16 v[64:67], a[128:131], v[160:163], v[64:67]\n"
        "ds_write_b128 v16, v[60:63] offset:19456\n"
        "v_bitop3_b32 v251, v173, v175, v249 bitop3:0x7e\n"
        "v_or3_b32 v252, v252, v250, v251\n"
        "v_mfma_f32_16x16x32_bf16 v[68:71], a[132:135], v[160:163], v[68:71]\n"
        "buffer_load_dwordx4 v[208:211], v9, s[36:39], s51 offen sc1\n"
        "v_bitop3_b32 v250, v176, v178, v248 bitop3:0x7e\n"
        "v_bitop3_b32 v251, v177, v179, v249 bitop3:0x7e\n"
        "v_mfma_f32_16x16x32_bf16 v[72:75], a[136:139], v[160:163], v[72:75]\n"
        "v_or3_b32 v252, v252, v250, v251\n"
        "v_mfma_f32_16x16x32_bf16 v[76:79], a[140:143], v[160:163], v[76:79]\n"
        "v_bitop3_b32 v250, v180, v182, v248 bitop3:0x7e\n"
        "v_bitop3_b32 v251, v181, v183, v249 bitop3:0x7e\n"
        "v_mfma_f32_16x16x32_bf16 v[64:67], a[144:147], v[164:167], v[64:67]\n"
        "buffer_load_dwordx4 v[212:215], v10, s[36:39], s51 offen sc1\n"
        "v_or3_b32 v252, v252, v250, v251\n"
        "v_and_b32 v252, s59, v252\n"
        "v_mfma_f32_16x16x32_bf16 v[68:71], a[148:151], v[164:167], v[68:71]\n"
        "v_cmp_ne_u32 vcc, 0, v252\n"
        "v_mfma_f32_16x16x32_bf16 v[72:75], a[152:155], v[164:167], v[72:75]\n"
        "s_waitcnt lgkmcnt(0)\n"
        "s_barrier\n"
        "v_mfma_f32_16x16x32_bf16 v[76:79], a[156:159], v[164:167], v[76:79]\n"
        "ds_read_b128 v[48:51], v17 offset:16384\n"
        "ds_read_b128 v[52:55], v17 offset:20480\n"
        "s_cbranch_vccnz .LX311_RETRY_%=\n"
        ".LX311_DONE_%=:\n"
        "v_mfma_f32_16x16x32_bf16 v[64:67], a[160:163], v[168:171], v[64:67]\n"
        "ds_read_b128 v[56:59], v17 offset:24576\n"
        "v_mfma_f32_16x16x32_bf16 v[68:71], a[164:167], v[168:171], v[68:71]\n"
        "ds_read_b128 v[60:63], v17 offset:28672\n"
        "v_mfma_f32_16x16x32_bf16 v[72:75], a[168:171], v[168:171], v[72:75]\n"
        "v_mfma_f32_16x16x32_bf16 v[76:79], a[172:175], v[168:171], v[76:79]\n"
        "v_mfma_f32_16x16x32_bf16 v[64:67], a[176:179], v[172:175], v[64:67]\n"
        "v_mfma_f32_16x16x32_bf16 v[68:71], a[180:183], v[172:175], v[68:71]\n"
        "s_waitcnt lgkmcnt(0)\n"
        "v_add_f32 v48, v48, v52\n"
        "s_waitcnt vmcnt(4)\n"
        "v_bitop3_b32 v250, v184, v186, v248 bitop3:0x7e\n"
        "v_mfma_f32_16x16x32_bf16 v[72:75], a[184:187], v[172:175], v[72:75]\n"
        "v_add_f32 v56, v56, v60\n"
        "v_add_f32 v48, v48, v56\n"
        "v_bitop3_b32 v251, v185, v187, v249 bitop3:0x7e\n"
        "v_or_b32 v252, v250, v251\n"
        "v_mfma_f32_16x16x32_bf16 v[76:79], a[188:191], v[172:175], v[76:79]\n"
        "v_fma_f32 v48, v48, v98, v108\n"
        "v_add_f32 v49, v49, v53\n"
        "v_bitop3_b32 v250, v188, v190, v248 bitop3:0x7e\n"
        "v_mfma_f32_16x16x32_bf16 v[64:67], a[192:195], v[176:179], v[64:67]\n"
        "v_add_f32 v57, v57, v61\n"
        "v_bitop3_b32 v251, v189, v191, v249 bitop3:0x7e\n"
        "v_or3_b32 v252, v252, v250, v251\n"
        "v_mfma_f32_16x16x32_bf16 v[68:71], a[196:199], v[176:179], v[68:71]\n"
        "v_add_f32 v49, v49, v57\n"
        "v_fma_f32 v49, v49, v98, v109\n"
        "v_bitop3_b32 v250, v192, v194, v248 bitop3:0x7e\n"
        "v_bitop3_b32 v251, v193, v195, v249 bitop3:0x7e\n"
        "v_mfma_f32_16x16x32_bf16 v[72:75], a[200:203], v[176:179], v[72:75]\n"
        "v_add_f32 v50, v50, v54\n"
        "v_add_f32 v58, v58, v62\n"
        "v_or3_b32 v252, v252, v250, v251\n"
        "v_mfma_f32_16x16x32_bf16 v[76:79], a[204:207], v[176:179], v[76:79]\n"
        "v_add_f32 v50, v50, v58\n"
        "v_fma_f32 v50, v50, v98, v110\n"
        "v_bitop3_b32 v250, v196, v198, v248 bitop3:0x7e\n"
        "v_bitop3_b32 v251, v197, v199, v249 bitop3:0x7e\n"
        "v_mfma_f32_16x16x32_bf16 v[64:67], a[208:211], v[180:183], v[64:67]\n"
        "v_add_f32 v51, v51, v55\n"
        "v_or3_b32 v252, v252, v250, v251\n"
        "v_and_b32 v252, s59, v252\n"
        "v_mfma_f32_16x16x32_bf16 v[68:71], a[212:215], v[180:183], v[68:71]\n"
        "v_add_f32 v59, v59, v63\n"
        "v_add_f32 v51, v51, v59\n"
        "v_cmp_ne_u32 vcc, 0, v252\n"
        "v_mfma_f32_16x16x32_bf16 v[72:75], a[216:219], v[180:183], v[72:75]\n"
        "v_fma_f32 v51, v51, v98, v111\n"
        "v_cvt_pk_bf16_f32 v52, v48, v49\n"
        "v_mfma_f32_16x16x32_bf16 v[76:79], a[220:223], v[180:183], v[76:79]\n"
        "v_cvt_pk_bf16_f32 v53, v50, v51\n"
        "s_cbranch_vccnz .LX312_RETRY_%=\n"
        ".LX312_DONE_%=:\n"
        "v_mfma_f32_16x16x32_bf16 v[80:83], a[96:99], v[184:187], v[80:83]\n"
        "v_mfma_f32_16x16x32_bf16 v[84:87], a[100:103], v[184:187], v[84:87]\n"
        "v_mfma_f32_16x16x32_bf16 v[88:91], a[104:107], v[184:187], v[88:91]\n"
        "v_mfma_f32_16x16x32_bf16 v[92:95], a[108:111], v[184:187], v[92:95]\n"
        "ds_write_b128 v13, v[64:67] offset:32768\n"
        "v_mfma_f32_16x16x32_bf16 v[80:83], a[112:115], v[188:191], v[80:83]\n"
        "ds_write_b128 v14, v[68:71] offset:33792\n"
        "v_mfma_f32_16x16x32_bf16 v[84:87], a[116:119], v[188:191], v[84:87]\n"
        "s_waitcnt vmcnt(0)\n"
        "v_bitop3_b32 v250, v200, v202, v248 bitop3:0x7e\n"
        "v_mfma_f32_16x16x32_bf16 v[88:91], a[120:123], v[188:191], v[88:91]\n"
        "ds_write_b128 v15, v[72:75] offset:34816\n"
        "v_bitop3_b32 v251, v201, v203, v249 bitop3:0x7e\n"
        "v_or_b32 v252, v250, v251\n"
        "v_mfma_f32_16x16x32_bf16 v[92:95], a[124:127], v[188:191], v[92:95]\n"
        "v_bitop3_b32 v250, v204, v206, v248 bitop3:0x7e\n"
        "v_mfma_f32_16x16x32_bf16 v[80:83], a[128:131], v[192:195], v[80:83]\n"
        "ds_write_b128 v16, v[76:79] offset:35840\n"
        "v_bitop3_b32 v251, v205, v207, v249 bitop3:0x7e\n"
        "v_or3_b32 v252, v252, v250, v251\n"
        "v_mfma_f32_16x16x32_bf16 v[84:87], a[132:135], v[192:195], v[84:87]\n"
        "v_bitop3_b32 v250, v208, v210, v248 bitop3:0x7e\n"
        "v_bitop3_b32 v251, v209, v211, v249 bitop3:0x7e\n"
        "v_mfma_f32_16x16x32_bf16 v[88:91], a[136:139], v[192:195], v[88:91]\n"
        "v_or3_b32 v252, v252, v250, v251\n"
        "v_mfma_f32_16x16x32_bf16 v[92:95], a[140:143], v[192:195], v[92:95]\n"
        "v_bitop3_b32 v250, v212, v214, v248 bitop3:0x7e\n"
        "v_bitop3_b32 v251, v213, v215, v249 bitop3:0x7e\n"
        "v_mfma_f32_16x16x32_bf16 v[80:83], a[144:147], v[196:199], v[80:83]\n"
        "v_or3_b32 v252, v252, v250, v251\n"
        "v_and_b32 v252, s59, v252\n"
        "v_mfma_f32_16x16x32_bf16 v[84:87], a[148:151], v[196:199], v[84:87]\n"
        "v_cmp_ne_u32 vcc, 0, v252\n"
        "v_mfma_f32_16x16x32_bf16 v[88:91], a[152:155], v[196:199], v[88:91]\n"
        "s_waitcnt lgkmcnt(0)\n"
        "s_barrier\n"
        "v_mfma_f32_16x16x32_bf16 v[92:95], a[156:159], v[196:199], v[92:95]\n"
        "ds_read_b128 v[64:67], v17 offset:32768\n"
        "ds_read_b128 v[68:71], v17 offset:36864\n"
        "s_cbranch_vccnz .LX313_RETRY_%=\n"
        ".LX313_DONE_%=:\n"
        "v_mfma_f32_16x16x32_bf16 v[80:83], a[160:163], v[200:203], v[80:83]\n"
        "ds_read_b128 v[72:75], v17 offset:40960\n"
        "v_mfma_f32_16x16x32_bf16 v[84:87], a[164:167], v[200:203], v[84:87]\n"
        "global_store_dwordx2 v18, v[36:37], s[34:35] sc0 sc1\n"
        "ds_read_b128 v[76:79], v17 offset:45056\n"
        "v_mfma_f32_16x16x32_bf16 v[88:91], a[168:171], v[200:203], v[88:91]\n"
        "v_mfma_f32_16x16x32_bf16 v[92:95], a[172:175], v[200:203], v[92:95]\n"
        "global_store_dwordx2 v18, v[52:53], s[34:35] offset:1024 sc0 sc1\n"
        "v_mfma_f32_16x16x32_bf16 v[80:83], a[176:179], v[204:207], v[80:83]\n"
        "v_mfma_f32_16x16x32_bf16 v[84:87], a[180:183], v[204:207], v[84:87]\n"
        "s_waitcnt lgkmcnt(0)\n"
        "v_add_f32 v64, v64, v68\n"
        "v_mfma_f32_16x16x32_bf16 v[88:91], a[184:187], v[204:207], v[88:91]\n"
        "v_add_f32 v72, v72, v76\n"
        "v_add_f32 v64, v64, v72\n"
        "v_mfma_f32_16x16x32_bf16 v[92:95], a[188:191], v[204:207], v[92:95]\n"
        "v_fma_f32 v64, v64, v24, v112\n"
        "v_add_f32 v65, v65, v69\n"
        "v_mfma_f32_16x16x32_bf16 v[80:83], a[192:195], v[208:211], v[80:83]\n"
        "v_add_f32 v73, v73, v77\n"
        "v_add_f32 v65, v65, v73\n"
        "v_mfma_f32_16x16x32_bf16 v[84:87], a[196:199], v[208:211], v[84:87]\n"
        "v_fma_f32 v65, v65, v24, v113\n"
        "v_add_f32 v66, v66, v70\n"
        "v_mfma_f32_16x16x32_bf16 v[88:91], a[200:203], v[208:211], v[88:91]\n"
        "v_add_f32 v74, v74, v78\n"
        "v_mfma_f32_16x16x32_bf16 v[92:95], a[204:207], v[208:211], v[92:95]\n"
        "v_add_f32 v66, v66, v74\n"
        "v_fma_f32 v66, v66, v24, v114\n"
        "v_mfma_f32_16x16x32_bf16 v[80:83], a[208:211], v[212:215], v[80:83]\n"
        "v_add_f32 v67, v67, v71\n"
        "v_add_f32 v75, v75, v79\n"
        "v_mfma_f32_16x16x32_bf16 v[84:87], a[212:215], v[212:215], v[84:87]\n"
        "v_add_f32 v67, v67, v75\n"
        "v_fma_f32 v67, v67, v24, v115\n"
        "v_mfma_f32_16x16x32_bf16 v[88:91], a[216:219], v[212:215], v[88:91]\n"
        "v_cvt_pk_bf16_f32 v68, v64, v65\n"
        "v_cvt_pk_bf16_f32 v69, v66, v67\n"
        "v_mfma_f32_16x16x32_bf16 v[92:95], a[220:223], v[212:215], v[92:95]\n"
        "global_store_dwordx2 v19, v[68:69], s[34:35] sc0 sc1\n"
        "ds_write_b128 v13, v[80:83] offset:49152\n"
        "ds_write_b128 v14, v[84:87] offset:50176\n"
        "s_nop 1\n"
        "ds_write_b128 v15, v[88:91] offset:51200\n"
        "s_nop 1\n"
        "ds_write_b128 v16, v[92:95] offset:52224\n"
        "s_waitcnt lgkmcnt(0)\n"
        "s_barrier\n"
        "ds_read_b128 v[80:83], v17 offset:49152\n"
        "ds_read_b128 v[84:87], v17 offset:53248\n"
        "ds_read_b128 v[88:91], v17 offset:57344\n"
        "ds_read_b128 v[92:95], v17 offset:61440\n"
        "s_and_b32 s59, s41, 7\n"
        "s_lshl_b32 s59, s59, 25\n"
        "s_sub_u32 s36, s36, s59\n"
        "s_subb_u32 s37, s37, 0\n"
        "s_or_b32 s49, s2, s40\n"
        "s_waitcnt lgkmcnt(0)\n"
        "v_pk_add_f32 v[80:81], v[80:81], v[84:85]\n"
        "v_pk_add_f32 v[88:89], v[88:89], v[92:93]\n"
        "v_pk_add_f32 v[80:81], v[80:81], v[88:89]\n"
        "v_pk_fma_f32 v[80:81], v[80:81], v[26:27], v[116:117] op_sel:[0,0,0] op_sel_hi:[1,0,1]\n"
        "v_pk_add_f32 v[82:83], v[82:83], v[86:87]\n"
        "v_pk_add_f32 v[90:91], v[90:91], v[94:95]\n"
        "v_pk_add_f32 v[82:83], v[82:83], v[90:91]\n"
        "v_pk_fma_f32 v[82:83], v[82:83], v[26:27], v[118:119] op_sel:[0,0,0] op_sel_hi:[1,0,1]\n"
        "v_cvt_pk_bf16_f32 v84, v80, v81\n"
        "v_cvt_pk_bf16_f32 v85, v82, v83\n"
        "global_store_dwordx2 v19, v[84:85], s[34:35] offset:1024 sc0 sc1\n"
        "s_cmp_lg_u32 s49, 0\n"
        "s_cbranch_scc1 .LNO_EPOCH_%=\n"
        "v_mov_b32 v32, 0\n"
        "v_mov_b32 v33, s41\n"
        "s_mov_b64 exec, 1\n"
        "buffer_store_dword v33, v32, s[36:39], 0 offen sc1\n"
        "s_mov_b64 exec, -1\n"
        ".LNO_EPOCH_%=:\n"
        "s_endpgm\n"
        ".LX1s_RETRY_%=:\n"
        "buffer_load_dwordx4 v[224:227], v8, s[36:39], s99 offen sc1\n"
        "buffer_load_dwordx4 v[228:231], v8, s[36:39], s99 offen offset:1024 sc1\n"
        "buffer_load_dwordx4 v[232:235], v8, s[36:39], s99 offen offset:2048 sc1\n"
        "buffer_load_dwordx4 v[236:239], v8, s[36:39], s99 offen offset:3072 sc1\n"
        "s_waitcnt vmcnt(0)\n"
        "v_bitop3_b32 v24, v225, v227, s41 bitop3:0x7e\n"
        "v_bitop3_b32 v25, v229, v231, s41 bitop3:0x7e\n"
        "v_bitop3_b32 v26, v233, v235, s41 bitop3:0x7e\n"
        "v_bitop3_b32 v27, v237, v239, s41 bitop3:0x7e\n"
        "v_or3_b32 v24, v24, v25, v26\n"
        "v_or_b32 v24, v24, v27\n"
        "v_cmp_ne_u32 vcc, 0, v24\n"
        "s_nop 0\n"
        "s_cbranch_vccz .LX1s_DONE_%=\n"
        "s_add_u32 s58, s58, 1\n"
        "s_cmp_lt_u32 s58, 0x10000\n"
        "s_cbranch_scc1 .LX1s_RETRY_%=\n"
        "s_branch .LX1s_DONE_%=\n"
        ".LX1x_RETRY_%=:\n"
        "buffer_load_dwordx4 v[240:243], v8, s[36:39], s98 offen sc1\n"
        "buffer_load_dwordx4 v[244:247], v8, s[36:39], s98 offen offset:1024 sc1\n"
        "buffer_load_dwordx4 v[248:251], v8, s[36:39], s98 offen offset:2048 sc1\n"
        "buffer_load_dwordx4 v[252:255], v8, s[36:39], s98 offen offset:3072 sc1\n"
        "s_waitcnt vmcnt(0)\n"
        "v_bitop3_b32 v28, v241, v243, s0 bitop3:0x7e\n"
        "v_bitop3_b32 v29, v245, v247, s0 bitop3:0x7e\n"
        "v_bitop3_b32 v30, v249, v251, s0 bitop3:0x7e\n"
        "v_bitop3_b32 v31, v253, v255, s0 bitop3:0x7e\n"
        "v_or3_b32 v28, v28, v29, v30\n"
        "v_or_b32 v28, v28, v31\n"
        "v_cmp_ne_u32 vcc, 0, v28\n"
        "s_nop 0\n"
        "s_cbranch_vccz .LX1x_DONE_%=\n"
        "s_add_u32 s58, s58, 1\n"
        "s_cmp_lt_u32 s58, 0x10000\n"
        "s_cbranch_scc1 .LX1x_RETRY_%=\n"
        "s_branch .LX1x_DONE_%=\n"
        ".LXW_RETRY_%=:\n"
        "s_lshl_b32 s51, s40, 12\n"
        "s_add_u32 s51, s51, 0x8000\n"
        "v_lshlrev_b32 v30, 6, v1\n"
        "buffer_load_dword v99, v30, s[36:39], s51 offen sc1\n"
        "s_waitcnt vmcnt(0)\n"
        "v_cmp_ne_u32 vcc, s41, v99\n"
        "s_nop 0\n"
        "s_cbranch_vccz .LXW_DONE_%=\n"
        "s_add_u32 s58, s58, 1\n"
        "s_cmp_lt_u32 s58, 0x10000\n"
        "s_cbranch_scc1 .LXW_RETRY_%=\n"
        "s_branch .LXW_DONE_%=\n"
        ".LX20_RETRY_%=:\n"
        "buffer_load_dwordx4 v[128:131], v0, s[36:39], s8 offen sc1\n"
        "buffer_load_dwordx4 v[132:135], v20, s[36:39], s8 offen sc1\n"
        "buffer_load_dwordx4 v[136:139], v40, s[36:39], s8 offen sc1\n"
        "buffer_load_dwordx4 v[140:143], v41, s[36:39], s8 offen sc1\n"
        "buffer_load_dwordx4 v[144:147], v0, s[36:39], s9 offen sc1\n"
        "buffer_load_dwordx4 v[148:151], v20, s[36:39], s9 offen sc1\n"
        "buffer_load_dwordx4 v[152:155], v40, s[36:39], s9 offen sc1\n"
        "buffer_load_dwordx4 v[156:159], v41, s[36:39], s9 offen sc1\n"
        "s_waitcnt vmcnt(0)\n"
        "v_max3_u32 v24, v129, v131, v133\n"
        "v_max_u32 v24, v24, v135\n"
        "v_cmp_le_u32 vcc, s1, v24\n"
        "s_nop 0\n"
        "s_cbranch_vccz .LX20_DONE_%=\n"
        "s_add_u32 s58, s58, 1\n"
        "s_cmp_lt_u32 s58, 0x10000\n"
        "s_cbranch_scc1 .LX20_RETRY_%=\n"
        "s_branch .LX20_DONE_%=\n"
        ".LX21_RETRY_%=:\n"
        "buffer_load_dwordx4 v[136:139], v40, s[36:39], s8 offen sc1\n"
        "buffer_load_dwordx4 v[140:143], v41, s[36:39], s8 offen sc1\n"
        "buffer_load_dwordx4 v[144:147], v0, s[36:39], s9 offen sc1\n"
        "buffer_load_dwordx4 v[148:151], v20, s[36:39], s9 offen sc1\n"
        "buffer_load_dwordx4 v[152:155], v40, s[36:39], s9 offen sc1\n"
        "buffer_load_dwordx4 v[156:159], v41, s[36:39], s9 offen sc1\n"
        "buffer_load_dwordx4 v[160:163], v0, s[36:39], s4 offen sc1\n"
        "buffer_load_dwordx4 v[164:167], v20, s[36:39], s4 offen sc1\n"
        "s_waitcnt vmcnt(0)\n"
        "v_max3_u32 v24, v137, v139, v141\n"
        "v_max_u32 v24, v24, v143\n"
        "v_cmp_le_u32 vcc, s1, v24\n"
        "s_nop 0\n"
        "s_cbranch_vccz .LX21_DONE_%=\n"
        "s_add_u32 s58, s58, 1\n"
        "s_cmp_lt_u32 s58, 0x10000\n"
        "s_cbranch_scc1 .LX21_RETRY_%=\n"
        "s_branch .LX21_DONE_%=\n"
        ".LX22_RETRY_%=:\n"
        "buffer_load_dwordx4 v[144:147], v0, s[36:39], s9 offen sc1\n"
        "buffer_load_dwordx4 v[148:151], v20, s[36:39], s9 offen sc1\n"
        "buffer_load_dwordx4 v[152:155], v40, s[36:39], s9 offen sc1\n"
        "buffer_load_dwordx4 v[156:159], v41, s[36:39], s9 offen sc1\n"
        "buffer_load_dwordx4 v[160:163], v0, s[36:39], s4 offen sc1\n"
        "buffer_load_dwordx4 v[164:167], v20, s[36:39], s4 offen sc1\n"
        "buffer_load_dwordx4 v[168:171], v40, s[36:39], s4 offen sc1\n"
        "buffer_load_dwordx4 v[172:175], v41, s[36:39], s4 offen sc1\n"
        "s_waitcnt vmcnt(0)\n"
        "v_max3_u32 v24, v145, v147, v149\n"
        "v_max3_u32 v24, v24, v151, v153\n"
        "v_max3_u32 v24, v24, v155, v157\n"
        "v_max_u32 v24, v24, v159\n"
        "v_cmp_le_u32 vcc, s1, v24\n"
        "s_nop 0\n"
        "s_cbranch_vccz .LX22_DONE_%=\n"
        "s_add_u32 s58, s58, 1\n"
        "s_cmp_lt_u32 s58, 0x10000\n"
        "s_cbranch_scc1 .LX22_RETRY_%=\n"
        "s_branch .LX22_DONE_%=\n"
        ".LX24_RETRY_%=:\n"
        "buffer_load_dwordx4 v[160:163], v0, s[36:39], s4 offen sc1\n"
        "buffer_load_dwordx4 v[164:167], v20, s[36:39], s4 offen sc1\n"
        "buffer_load_dwordx4 v[168:171], v40, s[36:39], s4 offen sc1\n"
        "buffer_load_dwordx4 v[172:175], v41, s[36:39], s4 offen sc1\n"
        "buffer_load_dwordx4 v[176:179], v0, s[36:39], s21 offen sc1\n"
        "buffer_load_dwordx4 v[180:183], v20, s[36:39], s21 offen sc1\n"
        "buffer_load_dwordx4 v[184:187], v40, s[36:39], s21 offen sc1\n"
        "buffer_load_dwordx4 v[188:191], v41, s[36:39], s21 offen sc1\n"
        "s_waitcnt vmcnt(0)\n"
        "v_max3_u32 v24, v161, v163, v165\n"
        "v_max3_u32 v24, v24, v167, v169\n"
        "v_max3_u32 v24, v24, v171, v173\n"
        "v_max_u32 v24, v24, v175\n"
        "v_cmp_le_u32 vcc, s1, v24\n"
        "s_nop 0\n"
        "s_cbranch_vccz .LX24_DONE_%=\n"
        "s_add_u32 s58, s58, 1\n"
        "s_cmp_lt_u32 s58, 0x10000\n"
        "s_cbranch_scc1 .LX24_RETRY_%=\n"
        "s_branch .LX24_DONE_%=\n"
        ".LX26_RETRY_%=:\n"
        "buffer_load_dwordx4 v[176:179], v0, s[36:39], s21 offen sc1\n"
        "buffer_load_dwordx4 v[180:183], v20, s[36:39], s21 offen sc1\n"
        "buffer_load_dwordx4 v[184:187], v40, s[36:39], s21 offen sc1\n"
        "buffer_load_dwordx4 v[188:191], v41, s[36:39], s21 offen sc1\n"
        "buffer_load_dwordx4 v[192:195], v0, s[36:39], s6 offen sc1\n"
        "buffer_load_dwordx4 v[196:199], v20, s[36:39], s6 offen sc1\n"
        "buffer_load_dwordx4 v[200:203], v40, s[36:39], s6 offen sc1\n"
        "buffer_load_dwordx4 v[204:207], v41, s[36:39], s6 offen sc1\n"
        "s_waitcnt vmcnt(0)\n"
        "v_max3_u32 v24, v177, v179, v181\n"
        "v_max3_u32 v24, v24, v183, v185\n"
        "v_max3_u32 v24, v24, v187, v189\n"
        "v_max_u32 v24, v24, v191\n"
        "v_cmp_le_u32 vcc, s1, v24\n"
        "s_nop 0\n"
        "s_cbranch_vccz .LX26_DONE_%=\n"
        "s_add_u32 s58, s58, 1\n"
        "s_cmp_lt_u32 s58, 0x10000\n"
        "s_cbranch_scc1 .LX26_RETRY_%=\n"
        "s_branch .LX26_DONE_%=\n"
        ".LX28_RETRY_%=:\n"
        "buffer_load_dwordx4 v[192:195], v0, s[36:39], s6 offen sc1\n"
        "buffer_load_dwordx4 v[196:199], v20, s[36:39], s6 offen sc1\n"
        "buffer_load_dwordx4 v[200:203], v40, s[36:39], s6 offen sc1\n"
        "buffer_load_dwordx4 v[204:207], v41, s[36:39], s6 offen sc1\n"
        "buffer_load_dwordx4 v[208:211], v0, s[36:39], s23 offen sc1\n"
        "buffer_load_dwordx4 v[212:215], v20, s[36:39], s23 offen sc1\n"
        "buffer_load_dwordx4 v[216:219], v40, s[36:39], s23 offen sc1\n"
        "buffer_load_dwordx4 v[220:223], v41, s[36:39], s23 offen sc1\n"
        "s_waitcnt vmcnt(0)\n"
        "v_max3_u32 v24, v193, v195, v197\n"
        "v_max3_u32 v24, v24, v199, v201\n"
        "v_max3_u32 v24, v24, v203, v205\n"
        "v_max_u32 v24, v24, v207\n"
        "v_cmp_le_u32 vcc, s1, v24\n"
        "s_nop 0\n"
        "s_cbranch_vccz .LX28_DONE_%=\n"
        "s_add_u32 s58, s58, 1\n"
        "s_cmp_lt_u32 s58, 0x10000\n"
        "s_cbranch_scc1 .LX28_RETRY_%=\n"
        "s_branch .LX28_DONE_%=\n"
        ".LX210_RETRY_%=:\n"
        "buffer_load_dwordx4 v[208:211], v0, s[36:39], s23 offen sc1\n"
        "buffer_load_dwordx4 v[212:215], v20, s[36:39], s23 offen sc1\n"
        "buffer_load_dwordx4 v[216:219], v40, s[36:39], s23 offen sc1\n"
        "buffer_load_dwordx4 v[220:223], v41, s[36:39], s23 offen sc1\n"
        "buffer_load_dwordx4 v[224:227], v0, s[36:39], s22 offen sc1\n"
        "buffer_load_dwordx4 v[228:231], v20, s[36:39], s22 offen sc1\n"
        "buffer_load_dwordx4 v[232:235], v40, s[36:39], s22 offen sc1\n"
        "buffer_load_dwordx4 v[236:239], v41, s[36:39], s22 offen sc1\n"
        "s_waitcnt vmcnt(0)\n"
        "v_max3_u32 v24, v209, v211, v213\n"
        "v_max3_u32 v24, v24, v215, v217\n"
        "v_max3_u32 v24, v24, v219, v221\n"
        "v_max_u32 v24, v24, v223\n"
        "v_cmp_le_u32 vcc, s1, v24\n"
        "s_nop 0\n"
        "s_cbranch_vccz .LX210_DONE_%=\n"
        "s_add_u32 s58, s58, 1\n"
        "s_cmp_lt_u32 s58, 0x10000\n"
        "s_cbranch_scc1 .LX210_RETRY_%=\n"
        "s_branch .LX210_DONE_%=\n"
        ".LX212_RETRY_%=:\n"
        "buffer_load_dwordx4 v[224:227], v0, s[36:39], s22 offen sc1\n"
        "buffer_load_dwordx4 v[228:231], v20, s[36:39], s22 offen sc1\n"
        "buffer_load_dwordx4 v[232:235], v40, s[36:39], s22 offen sc1\n"
        "buffer_load_dwordx4 v[236:239], v41, s[36:39], s22 offen sc1\n"
        "buffer_load_dwordx4 v[240:243], v0, s[36:39], s18 offen sc1\n"
        "buffer_load_dwordx4 v[244:247], v20, s[36:39], s18 offen sc1\n"
        "buffer_load_dwordx4 v[248:251], v40, s[36:39], s18 offen sc1\n"
        "buffer_load_dwordx4 v[252:255], v41, s[36:39], s18 offen sc1\n"
        "s_waitcnt vmcnt(0)\n"
        "v_max3_u32 v24, v225, v227, v229\n"
        "v_max3_u32 v24, v24, v231, v233\n"
        "v_max3_u32 v24, v24, v235, v237\n"
        "v_max_u32 v24, v24, v239\n"
        "v_cmp_le_u32 vcc, s1, v24\n"
        "s_nop 0\n"
        "s_cbranch_vccz .LX212_DONE_%=\n"
        "s_add_u32 s58, s58, 1\n"
        "s_cmp_lt_u32 s58, 0x10000\n"
        "s_cbranch_scc1 .LX212_RETRY_%=\n"
        "s_branch .LX212_DONE_%=\n"
        ".LX214_RETRY_%=:\n"
        "buffer_load_dwordx4 v[240:243], v0, s[36:39], s18 offen sc1\n"
        "buffer_load_dwordx4 v[244:247], v20, s[36:39], s18 offen sc1\n"
        "buffer_load_dwordx4 v[248:251], v40, s[36:39], s18 offen sc1\n"
        "buffer_load_dwordx4 v[252:255], v41, s[36:39], s18 offen sc1\n"
        "s_waitcnt vmcnt(0)\n"
        "v_max3_u32 v24, v241, v243, v245\n"
        "v_max3_u32 v24, v24, v247, v249\n"
        "v_max3_u32 v24, v24, v251, v253\n"
        "v_max_u32 v24, v24, v255\n"
        "v_cmp_le_u32 vcc, s1, v24\n"
        "s_nop 0\n"
        "s_cbranch_vccz .LX214_DONE_%=\n"
        "s_add_u32 s58, s58, 1\n"
        "s_cmp_lt_u32 s58, 0x10000\n"
        "s_cbranch_scc1 .LX214_RETRY_%=\n"
        "s_branch .LX214_DONE_%=\n"
        ".LX30_RETRY_%=:\n"
        "buffer_load_dwordx4 v[120:123], v7, s[36:39], s30 offen sc1\n"
        "buffer_load_dwordx4 v[124:127], v8, s[36:39], s30 offen sc1\n"
        "buffer_load_dwordx4 v[128:131], v9, s[36:39], s30 offen sc1\n"
        "buffer_load_dwordx4 v[132:135], v10, s[36:39], s30 offen sc1\n"
        "buffer_load_dwordx4 v[136:139], v7, s[36:39], s31 offen sc1\n"
        "buffer_load_dwordx4 v[140:143], v8, s[36:39], s31 offen sc1\n"
        "buffer_load_dwordx4 v[144:147], v9, s[36:39], s31 offen sc1\n"
        "buffer_load_dwordx4 v[148:151], v10, s[36:39], s31 offen sc1\n"
        "s_waitcnt vmcnt(0)\n"
        "v_bitop3_b32 v250, v120, v122, v248 bitop3:0x7e\n"
        "v_bitop3_b32 v251, v121, v123, v249 bitop3:0x7e\n"
        "v_or_b32 v252, v250, v251\n"
        "v_bitop3_b32 v250, v124, v126, v248 bitop3:0x7e\n"
        "v_bitop3_b32 v251, v125, v127, v249 bitop3:0x7e\n"
        "v_or3_b32 v252, v252, v250, v251\n"
        "v_bitop3_b32 v250, v128, v130, v248 bitop3:0x7e\n"
        "v_bitop3_b32 v251, v129, v131, v249 bitop3:0x7e\n"
        "v_or3_b32 v252, v252, v250, v251\n"
        "v_bitop3_b32 v250, v132, v134, v248 bitop3:0x7e\n"
        "v_bitop3_b32 v251, v133, v135, v249 bitop3:0x7e\n"
        "v_or3_b32 v252, v252, v250, v251\n"
        "v_and_b32 v252, s59, v252\n"
        "v_cmp_ne_u32 vcc, 0, v252\n"
        "s_nop 0\n"
        "s_cbranch_vccz .LX30_DONE_%=\n"
        "s_add_u32 s58, s58, 1\n"
        "s_cmp_lt_u32 s58, 0x10000\n"
        "s_cbranch_scc1 .LX30_RETRY_%=\n"
        "s_branch .LX30_DONE_%=\n"
        ".LX31_RETRY_%=:\n"
        "buffer_load_dwordx4 v[136:139], v7, s[36:39], s31 offen sc1\n"
        "buffer_load_dwordx4 v[140:143], v8, s[36:39], s31 offen sc1\n"
        "buffer_load_dwordx4 v[144:147], v9, s[36:39], s31 offen sc1\n"
        "buffer_load_dwordx4 v[148:151], v10, s[36:39], s31 offen sc1\n"
        "s_add_u32 s53, s30, 0x3000\n"
        "buffer_load_dwordx4 v[152:155], v4, s[36:39], s53 offen sc1\n"
        "buffer_load_dwordx4 v[156:159], v4, s[36:39], s53 offen offset:1024 sc1\n"
        "s_add_u32 s53, s31, 0x3000\n"
        "buffer_load_dwordx4 v[160:163], v4, s[36:39], s53 offen sc1\n"
        "buffer_load_dwordx4 v[164:167], v4, s[36:39], s53 offen offset:1024 sc1\n"
        "s_waitcnt vmcnt(0)\n"
        "v_bitop3_b32 v250, v136, v138, v248 bitop3:0x7e\n"
        "v_bitop3_b32 v251, v137, v139, v249 bitop3:0x7e\n"
        "v_or_b32 v252, v250, v251\n"
        "v_bitop3_b32 v250, v140, v142, v248 bitop3:0x7e\n"
        "v_bitop3_b32 v251, v141, v143, v249 bitop3:0x7e\n"
        "v_or3_b32 v252, v252, v250, v251\n"
        "v_bitop3_b32 v250, v144, v146, v248 bitop3:0x7e\n"
        "v_bitop3_b32 v251, v145, v147, v249 bitop3:0x7e\n"
        "v_or3_b32 v252, v252, v250, v251\n"
        "v_bitop3_b32 v250, v148, v150, v248 bitop3:0x7e\n"
        "v_bitop3_b32 v251, v149, v151, v249 bitop3:0x7e\n"
        "v_or3_b32 v252, v252, v250, v251\n"
        "v_and_b32 v252, s59, v252\n"
        "v_cmp_ne_u32 vcc, 0, v252\n"
        "s_nop 0\n"
        "s_cbranch_vccz .LX31_DONE_%=\n"
        "s_add_u32 s58, s58, 1\n"
        "s_cmp_lt_u32 s58, 0x10000\n"
        "s_cbranch_scc1 .LX31_RETRY_%=\n"
        "s_branch .LX31_DONE_%=\n"
        ".LX32_RETRY_%=:\n"
        "s_add_u32 s53, s30, 0x3000\n"
        "buffer_load_dwordx4 v[152:155], v4, s[36:39], s53 offen sc1\n"
        "buffer_load_dwordx4 v[156:159], v4, s[36:39], s53 offen offset:1024 sc1\n"
        "s_add_u32 s53, s31, 0x3000\n"
        "buffer_load_dwordx4 v[160:163], v4, s[36:39], s53 offen sc1\n"
        "buffer_load_dwordx4 v[164:167], v4, s[36:39], s53 offen offset:1024 sc1\n"
        "buffer_load_dwordx4 v[168:171], v7, s[36:39], s47 offen sc1\n"
        "buffer_load_dwordx4 v[172:175], v8, s[36:39], s47 offen sc1\n"
        "buffer_load_dwordx4 v[176:179], v9, s[36:39], s47 offen sc1\n"
        "buffer_load_dwordx4 v[180:183], v10, s[36:39], s47 offen sc1\n"
        "s_waitcnt vmcnt(0)\n"
        "v_bitop3_b32 v250, v152, v154, v248 bitop3:0x7e\n"
        "v_bitop3_b32 v251, v153, v155, v249 bitop3:0x7e\n"
        "v_or_b32 v252, v250, v251\n"
        "v_bitop3_b32 v250, v156, v158, v248 bitop3:0x7e\n"
        "v_bitop3_b32 v251, v157, v159, v249 bitop3:0x7e\n"
        "v_or3_b32 v252, v252, v250, v251\n"
        "v_bitop3_b32 v250, v160, v162, v248 bitop3:0x7e\n"
        "v_bitop3_b32 v251, v161, v163, v249 bitop3:0x7e\n"
        "v_or3_b32 v252, v252, v250, v251\n"
        "v_bitop3_b32 v250, v164, v166, v248 bitop3:0x7e\n"
        "v_bitop3_b32 v251, v165, v167, v249 bitop3:0x7e\n"
        "v_or3_b32 v252, v252, v250, v251\n"
        "v_and_b32 v252, s59, v252\n"
        "v_cmp_ne_u32 vcc, 0, v252\n"
        "s_nop 0\n"
        "s_cbranch_vccz .LX32_DONE_%=\n"
        "s_add_u32 s58, s58, 1\n"
        "s_cmp_lt_u32 s58, 0x10000\n"
        "s_cbranch_scc1 .LX32_RETRY_%=\n"
        "s_branch .LX32_DONE_%=\n"
        ".LXR_RETRY_%=:\n"
        "buffer_load_dwordx4 v[64:67], v0, s[36:39], s63 offen sc1\n"
        "buffer_load_dwordx4 v[68:71], v20, s[36:39], s63 offen sc1\n"
        "s_waitcnt vmcnt(0)\n"
        "v_max3_u32 v252, v65, v67, v69\n"
        "v_max_u32 v252, v252, v71\n"
        "v_cmp_le_u32 vcc, s1, v252\n"
        "s_nop 0\n"
        "s_cbranch_vccz .LXR_DONE_%=\n"
        "s_add_u32 s58, s58, 1\n"
        "s_cmp_lt_u32 s58, 0x10000\n"
        "s_cbranch_scc1 .LXR_RETRY_%=\n"
        "s_branch .LXR_DONE_%=\n"
        ".LX33_RETRY_%=:\n"
        "buffer_load_dwordx4 v[168:171], v7, s[36:39], s47 offen sc1\n"
        "buffer_load_dwordx4 v[172:175], v8, s[36:39], s47 offen sc1\n"
        "buffer_load_dwordx4 v[176:179], v9, s[36:39], s47 offen sc1\n"
        "buffer_load_dwordx4 v[180:183], v10, s[36:39], s47 offen sc1\n"
        "buffer_load_dwordx4 v[184:187], v7, s[36:39], s48 offen sc1\n"
        "buffer_load_dwordx4 v[188:191], v8, s[36:39], s48 offen sc1\n"
        "buffer_load_dwordx4 v[192:195], v9, s[36:39], s48 offen sc1\n"
        "buffer_load_dwordx4 v[196:199], v10, s[36:39], s48 offen sc1\n"
        "s_waitcnt vmcnt(0)\n"
        "v_bitop3_b32 v250, v168, v170, v248 bitop3:0x7e\n"
        "v_bitop3_b32 v251, v169, v171, v249 bitop3:0x7e\n"
        "v_or_b32 v252, v250, v251\n"
        "v_bitop3_b32 v250, v172, v174, v248 bitop3:0x7e\n"
        "v_bitop3_b32 v251, v173, v175, v249 bitop3:0x7e\n"
        "v_or3_b32 v252, v252, v250, v251\n"
        "v_bitop3_b32 v250, v176, v178, v248 bitop3:0x7e\n"
        "v_bitop3_b32 v251, v177, v179, v249 bitop3:0x7e\n"
        "v_or3_b32 v252, v252, v250, v251\n"
        "v_bitop3_b32 v250, v180, v182, v248 bitop3:0x7e\n"
        "v_bitop3_b32 v251, v181, v183, v249 bitop3:0x7e\n"
        "v_or3_b32 v252, v252, v250, v251\n"
        "v_and_b32 v252, s59, v252\n"
        "v_cmp_ne_u32 vcc, 0, v252\n"
        "s_nop 0\n"
        "s_cbranch_vccz .LX33_DONE_%=\n"
        "s_add_u32 s58, s58, 1\n"
        "s_cmp_lt_u32 s58, 0x10000\n"
        "s_cbranch_scc1 .LX33_RETRY_%=\n"
        "s_branch .LX33_DONE_%=\n"
        ".LX34_RETRY_%=:\n"
        "buffer_load_dwordx4 v[184:187], v7, s[36:39], s48 offen sc1\n"
        "buffer_load_dwordx4 v[188:191], v8, s[36:39], s48 offen sc1\n"
        "buffer_load_dwordx4 v[192:195], v9, s[36:39], s48 offen sc1\n"
        "buffer_load_dwordx4 v[196:199], v10, s[36:39], s48 offen sc1\n"
        "s_add_u32 s53, s47, 0x3000\n"
        "buffer_load_dwordx4 v[200:203], v4, s[36:39], s53 offen sc1\n"
        "buffer_load_dwordx4 v[204:207], v4, s[36:39], s53 offen offset:1024 sc1\n"
        "s_add_u32 s53, s48, 0x3000\n"
        "buffer_load_dwordx4 v[208:211], v4, s[36:39], s53 offen sc1\n"
        "buffer_load_dwordx4 v[212:215], v4, s[36:39], s53 offen offset:1024 sc1\n"
        "s_waitcnt vmcnt(0)\n"
        "v_bitop3_b32 v250, v184, v186, v248 bitop3:0x7e\n"
        "v_bitop3_b32 v251, v185, v187, v249 bitop3:0x7e\n"
        "v_or_b32 v252, v250, v251\n"
        "v_bitop3_b32 v250, v188, v190, v248 bitop3:0x7e\n"
        "v_bitop3_b32 v251, v189, v191, v249 bitop3:0x7e\n"
        "v_or3_b32 v252, v252, v250, v251\n"
        "v_bitop3_b32 v250, v192, v194, v248 bitop3:0x7e\n"
        "v_bitop3_b32 v251, v193, v195, v249 bitop3:0x7e\n"
        "v_or3_b32 v252, v252, v250, v251\n"
        "v_bitop3_b32 v250, v196, v198, v248 bitop3:0x7e\n"
        "v_bitop3_b32 v251, v197, v199, v249 bitop3:0x7e\n"
        "v_or3_b32 v252, v252, v250, v251\n"
        "v_and_b32 v252, s59, v252\n"
        "v_cmp_ne_u32 vcc, 0, v252\n"
        "s_nop 0\n"
        "s_cbranch_vccz .LX34_DONE_%=\n"
        "s_add_u32 s58, s58, 1\n"
        "s_cmp_lt_u32 s58, 0x10000\n"
        "s_cbranch_scc1 .LX34_RETRY_%=\n"
        "s_branch .LX34_DONE_%=\n"
        ".LX35_RETRY_%=:\n"
        "s_add_u32 s53, s47, 0x3000\n"
        "buffer_load_dwordx4 v[200:203], v4, s[36:39], s53 offen sc1\n"
        "buffer_load_dwordx4 v[204:207], v4, s[36:39], s53 offen offset:1024 sc1\n"
        "s_add_u32 s53, s48, 0x3000\n"
        "buffer_load_dwordx4 v[208:211], v4, s[36:39], s53 offen sc1\n"
        "buffer_load_dwordx4 v[212:215], v4, s[36:39], s53 offen offset:1024 sc1\n"
        "s_add_u32 s53, s30, 0x1000\n"
        "buffer_load_dwordx4 v[216:219], v7, s[36:39], s53 offen sc1\n"
        "buffer_load_dwordx4 v[220:223], v8, s[36:39], s53 offen sc1\n"
        "buffer_load_dwordx4 v[224:227], v9, s[36:39], s53 offen sc1\n"
        "buffer_load_dwordx4 v[228:231], v10, s[36:39], s53 offen sc1\n"
        "s_waitcnt vmcnt(0)\n"
        "v_bitop3_b32 v250, v200, v202, v248 bitop3:0x7e\n"
        "v_bitop3_b32 v251, v201, v203, v249 bitop3:0x7e\n"
        "v_or_b32 v252, v250, v251\n"
        "v_bitop3_b32 v250, v204, v206, v248 bitop3:0x7e\n"
        "v_bitop3_b32 v251, v205, v207, v249 bitop3:0x7e\n"
        "v_or3_b32 v252, v252, v250, v251\n"
        "v_bitop3_b32 v250, v208, v210, v248 bitop3:0x7e\n"
        "v_bitop3_b32 v251, v209, v211, v249 bitop3:0x7e\n"
        "v_or3_b32 v252, v252, v250, v251\n"
        "v_bitop3_b32 v250, v212, v214, v248 bitop3:0x7e\n"
        "v_bitop3_b32 v251, v213, v215, v249 bitop3:0x7e\n"
        "v_or3_b32 v252, v252, v250, v251\n"
        "v_and_b32 v252, s59, v252\n"
        "v_cmp_ne_u32 vcc, 0, v252\n"
        "s_nop 0\n"
        "s_cbranch_vccz .LX35_DONE_%=\n"
        "s_add_u32 s58, s58, 1\n"
        "s_cmp_lt_u32 s58, 0x10000\n"
        "s_cbranch_scc1 .LX35_RETRY_%=\n"
        "s_branch .LX35_DONE_%=\n"
        ".LX36_RETRY_%=:\n"
        "s_add_u32 s53, s30, 0x1000\n"
        "buffer_load_dwordx4 v[216:219], v7, s[36:39], s53 offen sc1\n"
        "buffer_load_dwordx4 v[220:223], v8, s[36:39], s53 offen sc1\n"
        "buffer_load_dwordx4 v[224:227], v9, s[36:39], s53 offen sc1\n"
        "buffer_load_dwordx4 v[228:231], v10, s[36:39], s53 offen sc1\n"
        "s_add_u32 s53, s30, 0x2000\n"
        "buffer_load_dwordx4 v[232:235], v7, s[36:39], s53 offen sc1\n"
        "buffer_load_dwordx4 v[236:239], v8, s[36:39], s53 offen sc1\n"
        "buffer_load_dwordx4 v[240:243], v9, s[36:39], s53 offen sc1\n"
        "buffer_load_dwordx4 v[244:247], v10, s[36:39], s53 offen sc1\n"
        "s_waitcnt vmcnt(0)\n"
        "v_bitop3_b32 v250, v216, v218, v248 bitop3:0x7e\n"
        "v_bitop3_b32 v251, v217, v219, v249 bitop3:0x7e\n"
        "v_or_b32 v252, v250, v251\n"
        "v_bitop3_b32 v250, v220, v222, v248 bitop3:0x7e\n"
        "v_bitop3_b32 v251, v221, v223, v249 bitop3:0x7e\n"
        "v_or3_b32 v252, v252, v250, v251\n"
        "v_bitop3_b32 v250, v224, v226, v248 bitop3:0x7e\n"
        "v_bitop3_b32 v251, v225, v227, v249 bitop3:0x7e\n"
        "v_or3_b32 v252, v252, v250, v251\n"
        "v_bitop3_b32 v250, v228, v230, v248 bitop3:0x7e\n"
        "v_bitop3_b32 v251, v229, v231, v249 bitop3:0x7e\n"
        "v_or3_b32 v252, v252, v250, v251\n"
        "v_and_b32 v252, s59, v252\n"
        "v_cmp_ne_u32 vcc, 0, v252\n"
        "s_nop 0\n"
        "s_cbranch_vccz .LX36_DONE_%=\n"
        "s_add_u32 s58, s58, 1\n"
        "s_cmp_lt_u32 s58, 0x10000\n"
        "s_cbranch_scc1 .LX36_RETRY_%=\n"
        "s_branch .LX36_DONE_%=\n"
        ".LX37_RETRY_%=:\n"
        "s_add_u32 s53, s30, 0x2000\n"
        "buffer_load_dwordx4 v[232:235], v7, s[36:39], s53 offen sc1\n"
        "buffer_load_dwordx4 v[236:239], v8, s[36:39], s53 offen sc1\n"
        "buffer_load_dwordx4 v[240:243], v9, s[36:39], s53 offen sc1\n"
        "buffer_load_dwordx4 v[244:247], v10, s[36:39], s53 offen sc1\n"
        "s_add_u32 s53, s31, 0x1000\n"
        "buffer_load_dwordx4 v[120:123], v7, s[36:39], s53 offen sc1\n"
        "buffer_load_dwordx4 v[124:127], v8, s[36:39], s53 offen sc1\n"
        "buffer_load_dwordx4 v[128:131], v9, s[36:39], s53 offen sc1\n"
        "buffer_load_dwordx4 v[132:135], v10, s[36:39], s53 offen sc1\n"
        "s_waitcnt vmcnt(0)\n"
        "v_bitop3_b32 v250, v232, v234, v248 bitop3:0x7e\n"
        "v_bitop3_b32 v251, v233, v235, v249 bitop3:0x7e\n"
        "v_or_b32 v252, v250, v251\n"
        "v_bitop3_b32 v250, v236, v238, v248 bitop3:0x7e\n"
        "v_bitop3_b32 v251, v237, v239, v249 bitop3:0x7e\n"
        "v_or3_b32 v252, v252, v250, v251\n"
        "v_bitop3_b32 v250, v240, v242, v248 bitop3:0x7e\n"
        "v_bitop3_b32 v251, v241, v243, v249 bitop3:0x7e\n"
        "v_or3_b32 v252, v252, v250, v251\n"
        "v_bitop3_b32 v250, v244, v246, v248 bitop3:0x7e\n"
        "v_bitop3_b32 v251, v245, v247, v249 bitop3:0x7e\n"
        "v_or3_b32 v252, v252, v250, v251\n"
        "v_and_b32 v252, s59, v252\n"
        "v_cmp_ne_u32 vcc, 0, v252\n"
        "s_nop 0\n"
        "s_cbranch_vccz .LX37_DONE_%=\n"
        "s_add_u32 s58, s58, 1\n"
        "s_cmp_lt_u32 s58, 0x10000\n"
        "s_cbranch_scc1 .LX37_RETRY_%=\n"
        "s_branch .LX37_DONE_%=\n"
        ".LXS_RETRY_%=:\n"
        "buffer_load_dwordx4 v[96:99], v11, s[36:39], 0 offen sc1\n"
        "buffer_load_dwordx4 v[24:27], v21, s[36:39], 0 offen sc1\n"
        "s_waitcnt vmcnt(0)\n"
        "v_bitop3_b32 v250, v97, v99, s41 bitop3:0x7e\n"
        "v_bitop3_b32 v251, v25, v27, s41 bitop3:0x7e\n"
        "v_or_b32 v252, v250, v251\n"
        "v_cmp_ne_u32 vcc, 0, v252\n"
        "s_nop 0\n"
        "s_cbranch_vccz .LXS_DONE_%=\n"
        "s_add_u32 s58, s58, 1\n"
        "s_cmp_lt_u32 s58, 0x10000\n"
        "s_cbranch_scc1 .LXS_RETRY_%=\n"
        "s_branch .LXS_DONE_%=\n"
        ".LX38_RETRY_%=:\n"
        "s_add_u32 s53, s31, 0x1000\n"
        "buffer_load_dwordx4 v[120:123], v7, s[36:39], s53 offen sc1\n"
        "buffer_load_dwordx4 v[124:127], v8, s[36:39], s53 offen sc1\n"
        "buffer_load_dwordx4 v[128:131], v9, s[36:39], s53 offen sc1\n"
        "buffer_load_dwordx4 v[132:135], v10, s[36:39], s53 offen sc1\n"
        "s_add_u32 s53, s31, 0x2000\n"
        "buffer_load_dwordx4 v[136:139], v7, s[36:39], s53 offen sc1\n"
        "buffer_load_dwordx4 v[140:143], v8, s[36:39], s53 offen sc1\n"
        "buffer_load_dwordx4 v[144:147], v9, s[36:39], s53 offen sc1\n"
        "buffer_load_dwordx4 v[148:151], v10, s[36:39], s53 offen sc1\n"
        "s_waitcnt vmcnt(0)\n"
        "v_bitop3_b32 v250, v120, v122, v248 bitop3:0x7e\n"
        "v_bitop3_b32 v251, v121, v123, v249 bitop3:0x7e\n"
        "v_or_b32 v252, v250, v251\n"
        "v_bitop3_b32 v250, v124, v126, v248 bitop3:0x7e\n"
        "v_bitop3_b32 v251, v125, v127, v249 bitop3:0x7e\n"
        "v_or3_b32 v252, v252, v250, v251\n"
        "v_bitop3_b32 v250, v128, v130, v248 bitop3:0x7e\n"
        "v_bitop3_b32 v251, v129, v131, v249 bitop3:0x7e\n"
        "v_or3_b32 v252, v252, v250, v251\n"
        "v_bitop3_b32 v250, v132, v134, v248 bitop3:0x7e\n"
        "v_bitop3_b32 v251, v133, v135, v249 bitop3:0x7e\n"
        "v_or3_b32 v252, v252, v250, v251\n"
        "v_and_b32 v252, s59, v252\n"
        "v_cmp_ne_u32 vcc, 0, v252\n"
        "s_nop 0\n"
        "s_cbranch_vccz .LX38_DONE_%=\n"
        "s_add_u32 s58, s58, 1\n"
        "s_cmp_lt_u32 s58, 0x10000\n"
        "s_cbranch_scc1 .LX38_RETRY_%=\n"
        "s_branch .LX38_DONE_%=\n"
        ".LX39_RETRY_%=:\n"
        "s_add_u32 s53, s31, 0x2000\n"
        "buffer_load_dwordx4 v[136:139], v7, s[36:39], s53 offen sc1\n"
        "buffer_load_dwordx4 v[140:143], v8, s[36:39], s53 offen sc1\n"
        "buffer_load_dwordx4 v[144:147], v9, s[36:39], s53 offen sc1\n"
        "buffer_load_dwordx4 v[148:151], v10, s[36:39], s53 offen sc1\n"
        "s_add_u32 s53, s47, 0x1000\n"
        "buffer_load_dwordx4 v[152:155], v7, s[36:39], s53 offen sc1\n"
        "buffer_load_dwordx4 v[156:159], v8, s[36:39], s53 offen sc1\n"
        "buffer_load_dwordx4 v[160:163], v9, s[36:39], s53 offen sc1\n"
        "buffer_load_dwordx4 v[164:167], v10, s[36:39], s53 offen sc1\n"
        "s_waitcnt vmcnt(0)\n"
        "v_bitop3_b32 v250, v136, v138, v248 bitop3:0x7e\n"
        "v_bitop3_b32 v251, v137, v139, v249 bitop3:0x7e\n"
        "v_or_b32 v252, v250, v251\n"
        "v_bitop3_b32 v250, v140, v142, v248 bitop3:0x7e\n"
        "v_bitop3_b32 v251, v141, v143, v249 bitop3:0x7e\n"
        "v_or3_b32 v252, v252, v250, v251\n"
        "v_bitop3_b32 v250, v144, v146, v248 bitop3:0x7e\n"
        "v_bitop3_b32 v251, v145, v147, v249 bitop3:0x7e\n"
        "v_or3_b32 v252, v252, v250, v251\n"
        "v_bitop3_b32 v250, v148, v150, v248 bitop3:0x7e\n"
        "v_bitop3_b32 v251, v149, v151, v249 bitop3:0x7e\n"
        "v_or3_b32 v252, v252, v250, v251\n"
        "v_and_b32 v252, s59, v252\n"
        "v_cmp_ne_u32 vcc, 0, v252\n"
        "s_nop 0\n"
        "s_cbranch_vccz .LX39_DONE_%=\n"
        "s_add_u32 s58, s58, 1\n"
        "s_cmp_lt_u32 s58, 0x10000\n"
        "s_cbranch_scc1 .LX39_RETRY_%=\n"
        "s_branch .LX39_DONE_%=\n"
        ".LX310_RETRY_%=:\n"
        "s_add_u32 s53, s47, 0x1000\n"
        "buffer_load_dwordx4 v[152:155], v7, s[36:39], s53 offen sc1\n"
        "buffer_load_dwordx4 v[156:159], v8, s[36:39], s53 offen sc1\n"
        "buffer_load_dwordx4 v[160:163], v9, s[36:39], s53 offen sc1\n"
        "buffer_load_dwordx4 v[164:167], v10, s[36:39], s53 offen sc1\n"
        "s_add_u32 s53, s47, 0x2000\n"
        "buffer_load_dwordx4 v[168:171], v7, s[36:39], s53 offen sc1\n"
        "buffer_load_dwordx4 v[172:175], v8, s[36:39], s53 offen sc1\n"
        "buffer_load_dwordx4 v[176:179], v9, s[36:39], s53 offen sc1\n"
        "buffer_load_dwordx4 v[180:183], v10, s[36:39], s53 offen sc1\n"
        "s_waitcnt vmcnt(0)\n"
        "v_bitop3_b32 v250, v152, v154, v248 bitop3:0x7e\n"
        "v_bitop3_b32 v251, v153, v155, v249 bitop3:0x7e\n"
        "v_or_b32 v252, v250, v251\n"
        "v_bitop3_b32 v250, v156, v158, v248 bitop3:0x7e\n"
        "v_bitop3_b32 v251, v157, v159, v249 bitop3:0x7e\n"
        "v_or3_b32 v252, v252, v250, v251\n"
        "v_bitop3_b32 v250, v160, v162, v248 bitop3:0x7e\n"
        "v_bitop3_b32 v251, v161, v163, v249 bitop3:0x7e\n"
        "v_or3_b32 v252, v252, v250, v251\n"
        "v_bitop3_b32 v250, v164, v166, v248 bitop3:0x7e\n"
        "v_bitop3_b32 v251, v165, v167, v249 bitop3:0x7e\n"
        "v_or3_b32 v252, v252, v250, v251\n"
        "v_and_b32 v252, s59, v252\n"
        "v_cmp_ne_u32 vcc, 0, v252\n"
        "s_nop 0\n"
        "s_cbranch_vccz .LX310_DONE_%=\n"
        "s_add_u32 s58, s58, 1\n"
        "s_cmp_lt_u32 s58, 0x10000\n"
        "s_cbranch_scc1 .LX310_RETRY_%=\n"
        "s_branch .LX310_DONE_%=\n"
        ".LX311_RETRY_%=:\n"
        "s_add_u32 s53, s47, 0x2000\n"
        "buffer_load_dwordx4 v[168:171], v7, s[36:39], s53 offen sc1\n"
        "buffer_load_dwordx4 v[172:175], v8, s[36:39], s53 offen sc1\n"
        "buffer_load_dwordx4 v[176:179], v9, s[36:39], s53 offen sc1\n"
        "buffer_load_dwordx4 v[180:183], v10, s[36:39], s53 offen sc1\n"
        "s_add_u32 s53, s48, 0x1000\n"
        "buffer_load_dwordx4 v[184:187], v7, s[36:39], s53 offen sc1\n"
        "buffer_load_dwordx4 v[188:191], v8, s[36:39], s53 offen sc1\n"
        "buffer_load_dwordx4 v[192:195], v9, s[36:39], s53 offen sc1\n"
        "buffer_load_dwordx4 v[196:199], v10, s[36:39], s53 offen sc1\n"
        "s_waitcnt vmcnt(0)\n"
        "v_bitop3_b32 v250, v168, v170, v248 bitop3:0x7e\n"
        "v_bitop3_b32 v251, v169, v171, v249 bitop3:0x7e\n"
        "v_or_b32 v252, v250, v251\n"
        "v_bitop3_b32 v250, v172, v174, v248 bitop3:0x7e\n"
        "v_bitop3_b32 v251, v173, v175, v249 bitop3:0x7e\n"
        "v_or3_b32 v252, v252, v250, v251\n"
        "v_bitop3_b32 v250, v176, v178, v248 bitop3:0x7e\n"
        "v_bitop3_b32 v251, v177, v179, v249 bitop3:0x7e\n"
        "v_or3_b32 v252, v252, v250, v251\n"
        "v_bitop3_b32 v250, v180, v182, v248 bitop3:0x7e\n"
        "v_bitop3_b32 v251, v181, v183, v249 bitop3:0x7e\n"
        "v_or3_b32 v252, v252, v250, v251\n"
        "v_and_b32 v252, s59, v252\n"
        "v_cmp_ne_u32 vcc, 0, v252\n"
        "s_nop 0\n"
        "s_cbranch_vccz .LX311_DONE_%=\n"
        "s_add_u32 s58, s58, 1\n"
        "s_cmp_lt_u32 s58, 0x10000\n"
        "s_cbranch_scc1 .LX311_RETRY_%=\n"
        "s_branch .LX311_DONE_%=\n"
        ".LX312_RETRY_%=:\n"
        "s_add_u32 s53, s48, 0x1000\n"
        "buffer_load_dwordx4 v[184:187], v7, s[36:39], s53 offen sc1\n"
        "buffer_load_dwordx4 v[188:191], v8, s[36:39], s53 offen sc1\n"
        "buffer_load_dwordx4 v[192:195], v9, s[36:39], s53 offen sc1\n"
        "buffer_load_dwordx4 v[196:199], v10, s[36:39], s53 offen sc1\n"
        "s_add_u32 s53, s48, 0x2000\n"
        "buffer_load_dwordx4 v[200:203], v7, s[36:39], s53 offen sc1\n"
        "buffer_load_dwordx4 v[204:207], v8, s[36:39], s53 offen sc1\n"
        "buffer_load_dwordx4 v[208:211], v9, s[36:39], s53 offen sc1\n"
        "buffer_load_dwordx4 v[212:215], v10, s[36:39], s53 offen sc1\n"
        "s_waitcnt vmcnt(0)\n"
        "v_bitop3_b32 v250, v184, v186, v248 bitop3:0x7e\n"
        "v_bitop3_b32 v251, v185, v187, v249 bitop3:0x7e\n"
        "v_or_b32 v252, v250, v251\n"
        "v_bitop3_b32 v250, v188, v190, v248 bitop3:0x7e\n"
        "v_bitop3_b32 v251, v189, v191, v249 bitop3:0x7e\n"
        "v_or3_b32 v252, v252, v250, v251\n"
        "v_bitop3_b32 v250, v192, v194, v248 bitop3:0x7e\n"
        "v_bitop3_b32 v251, v193, v195, v249 bitop3:0x7e\n"
        "v_or3_b32 v252, v252, v250, v251\n"
        "v_bitop3_b32 v250, v196, v198, v248 bitop3:0x7e\n"
        "v_bitop3_b32 v251, v197, v199, v249 bitop3:0x7e\n"
        "v_or3_b32 v252, v252, v250, v251\n"
        "v_and_b32 v252, s59, v252\n"
        "v_cmp_ne_u32 vcc, 0, v252\n"
        "s_nop 0\n"
        "s_cbranch_vccz .LX312_DONE_%=\n"
        "s_add_u32 s58, s58, 1\n"
        "s_cmp_lt_u32 s58, 0x10000\n"
        "s_cbranch_scc1 .LX312_RETRY_%=\n"
        "s_branch .LX312_DONE_%=\n"
        ".LX313_RETRY_%=:\n"
        "s_add_u32 s53, s48, 0x2000\n"
        "buffer_load_dwordx4 v[200:203], v7, s[36:39], s53 offen sc1\n"
        "buffer_load_dwordx4 v[204:207], v8, s[36:39], s53 offen sc1\n"
        "buffer_load_dwordx4 v[208:211], v9, s[36:39], s53 offen sc1\n"
        "buffer_load_dwordx4 v[212:215], v10, s[36:39], s53 offen sc1\n"
        "s_waitcnt vmcnt(0)\n"
        "v_bitop3_b32 v250, v200, v202, v248 bitop3:0x7e\n"
        "v_bitop3_b32 v251, v201, v203, v249 bitop3:0x7e\n"
        "v_or_b32 v252, v250, v251\n"
        "v_bitop3_b32 v250, v204, v206, v248 bitop3:0x7e\n"
        "v_bitop3_b32 v251, v205, v207, v249 bitop3:0x7e\n"
        "v_or3_b32 v252, v252, v250, v251\n"
        "v_bitop3_b32 v250, v208, v210, v248 bitop3:0x7e\n"
        "v_bitop3_b32 v251, v209, v211, v249 bitop3:0x7e\n"
        "v_or3_b32 v252, v252, v250, v251\n"
        "v_bitop3_b32 v250, v212, v214, v248 bitop3:0x7e\n"
        "v_bitop3_b32 v251, v213, v215, v249 bitop3:0x7e\n"
        "v_or3_b32 v252, v252, v250, v251\n"
        "v_and_b32 v252, s59, v252\n"
        "v_cmp_ne_u32 vcc, 0, v252\n"
        "s_nop 0\n"
        "s_cbranch_vccz .LX313_DONE_%=\n"
        "s_add_u32 s58, s58, 1\n"
        "s_cmp_lt_u32 s58, 0x10000\n"
        "s_cbranch_scc1 .LX313_RETRY_%=\n"
        "s_branch .LX313_DONE_%=\n"
        :
        : "{s[0:1]}"(__builtin_amdgcn_kernarg_segment_ptr()), "{s2}"(__builtin_amdgcn_workgroup_id_x()),
          "{v0}"(__builtin_amdgcn_workitem_id_x()), "i"((__attribute__((address_space(3))) char*)smem)
        : "v1", "v2", "v3", "v4", "v5", "v6", "v7", "v8", "v9", "v10", "v11", "v12", "v13", "v14", "v15", "v16", "v17", "v18", "v19", "v20", "v21", "v22", "v23", "v24", "v25", "v26", "v27", "v28", "v29", "v30", "v31", "v32", "v33", "v34", "v35", "v36", "v37", "v38", "v39", "v40", "v41", "v42", "v43", "v44", "v45", "v46", "v47", "v48", "v49", "v50", "v51", "v52", "v53", "v54", "v55", "v56", "v57", "v58", "v59", "v60", "v61", "v62", "v63", "v64", "v65", "v66", "v67", "v68", "v69", "v70", "v71", "v72", "v73", "v74", "v75", "v76", "v77", "v78", "v79", "v80", "v81", "v82", "v83", "v84", "v85", "v86", "v87", "v88", "v89", "v90", "v91", "v92", "v93", "v94", "v95", "v96", "v97", "v98", "v99", "v100", "v101", "v102", "v103", "v104", "v105", "v106", "v107", "v108", "v109", "v110", "v111", "v112", "v113", "v114", "v115", "v116", "v117", "v118", "v119", "v120", "v121", "v122", "v123", "v124", "v125", "v126", "v127", "v128", "v129", "v130", "v131", "v132", "v133", "v134", "v135", "v136", "v137", "v138", "v139", "v140", "v141", "v142", "v143", "v144", "v145", "v146", "v147", "v148", "v149", "v150", "v151", "v152", "v153", "v154", "v155", "v156", "v157", "v158", "v159", "v160", "v161", "v162", "v163", "v164", "v165", "v166", "v167", "v168", "v169", "v170", "v171", "v172", "v173", "v174", "v175", "v176", "v177", "v178", "v179", "v180", "v181", "v182", "v183", "v184", "v185", "v186", "v187", "v188", "v189", "v190", "v191", "v192", "v193", "v194", "v195", "v196", "v197", "v198", "v199", "v200", "v201", "v202", "v203", "v204", "v205", "v206", "v207", "v208", "v209", "v210", "v211", "v212", "v213", "v214", "v215", "v216", "v217", "v218", "v219", "v220", "v221", "v222", "v223", "v224", "v225", "v226", "v227", "v228", "v229", "v230", "v231", "v232", "v233", "v234", "v235", "v236", "v237", "v238", "v239", "v240", "v241", "v242", "v243", "v244", "v245", "v246", "v247", "v248", "v249", "v250", "v251", "v252", "v253", "v254", "v255", "a0", "a1", "a2", "a3", "a4", "a5", "a6", "a7", "a8", "a9", "a10", "a11", "a12", "a13", "a14", "a15", "a16", "a17", "a18", "a19", "a20", "a21", "a22", "a23", "a24", "a25", "a26", "a27", "a28", "a29", "a30", "a31", "a32", "a33", "a34", "a35", "a36", "a37", "a38", "a39", "a40", "a41", "a42", "a43", "a44", "a45", "a46", "a47", "a48", "a49", "a50", "a51", "a52", "a53", "a54", "a55", "a56", "a57", "a58", "a59", "a60", "a61", "a62", "a63", "a64", "a65", "a66", "a67", "a68", "a69", "a70", "a71", "a72", "a73", "a74", "a75", "a76", "a77", "a78", "a79", "a80", "a81", "a82", "a83", "a84", "a85", "a86", "a87", "a88", "a89", "a90", "a91", "a92", "a93", "a94", "a95", "a96", "a97", "a98", "a99", "a100", "a101", "a102", "a103", "a104", "a105", "a106", "a107", "a108", "a109", "a110", "a111", "a112", "a113", "a114", "a115", "a116", "a117", "a118", "a119", "a120", "a121", "a122", "a123", "a124", "a125", "a126", "a127", "a128", "a129", "a130", "a131", "a132", "a133", "a134", "a135", "a136", "a137", "a138", "a139", "a140", "a141", "a142", "a143", "a144", "a145", "a146", "a147", "a148", "a149", "a150", "a151", "a152", "a153", "a154", "a155", "a156", "a157", "a158", "a159", "a160", "a161", "a162", "a163", "a164", "a165", "a166", "a167", "a168", "a169", "a170", "a171", "a172", "a173", "a174", "a175", "a176", "a177", "a178", "a179", "a180", "a181", "a182", "a183", "a184", "a185", "a186", "a187", "a188", "a189", "a190", "a191", "a192", "a193", "a194", "a195", "a196", "a197", "a198", "a199", "a200", "a201", "a202", "a203", "a204", "a205", "a206", "a207", "a208", "a209", "a210", "a211", "a212", "a213", "a214", "a215", "a216", "a217", "a218", "a219", "a220", "a221", "a222", "a223", "a224", "a225", "a226", "a227", "a228", "a229", "a230", "a231", "a232", "a233", "a234", "a235", "a236", "a237", "a238", "a239", "a240", "a241", "a242", "a243", "a244", "a245", "a246", "a247", "a248", "a249", "a250", "a251", "a252", "a253", "a254", "a255", "s3", "s4", "s5", "s6", "s7", "s8", "s9", "s10", "s11", "s12", "s13", "s14", "s15", "s16", "s17", "s18", "s19", "s20", "s21", "s22", "s23", "s24", "s25", "s26", "s27", "s28", "s29", "s30", "s31", "s33", "s34", "s35", "s36", "s37", "s38", "s39", "s40", "s41", "s42", "s43", "s44", "s45", "s46", "s47", "s48", "s49", "s50", "s51", "s52", "s53", "s54", "s55", "s56", "s57", "s58", "s59", "s60", "s61", "s62", "s63", "s64", "s65", "s66", "s67", "s68", "s69", "s70", "s71", "s72", "s73", "s74", "s75", "s76", "s77", "s78", "s79", "s80", "s81", "s82", "s83", "s84", "s85", "s86", "s87", "s88", "s89", "s90", "s91", "s92", "s93", "s94", "s95", "s96", "s97", "s98", "s99", "vcc", "scc", "memory");
#endif
}

extern "C" void kernel_launch(void* const* d_in, const int* in_sizes, int n_in, void* d_out, int out_size, void* d_ws, size_t ws_size,
                              hipStream_t stream) {
    fused_kernel<<<256, 256, 0, stream>>>((const u16*)d_in[0], (const u16*)d_in[1], (const u16*)d_in[2], (const u16*)d_in[3], (const u16*)d_in[4],
                                          (const u16*)d_in[5], (const u16*)d_in[6], (const u16*)d_in[7], (const u16*)d_in[8], (const u16*)d_in[10],
                                          (const u16*)d_in[11], (const u16*)d_in[12], (const u16*)d_in[13], (u16*)d_out, (char*)d_ws);
}
```
